```python
import torch

def _softmax(x, axis=-1):
    return torch.softmax(x, dim=axis)
def _gelu(x):
    return torch.nn.functional.gelu(x, approximate="tanh")
def _silu(x):
    return torch.nn.functional.silu(x)
def _sigmoid(x):
    return torch.sigmoid(x)
def _layernorm(x, axis=-1, eps=1e-5):
    m = x.mean(dim=axis, keepdim=True)
    v = x.var(dim=axis, keepdim=True, unbiased=False)
    return (x - m) / torch.sqrt(v + eps)
def _scatter_add(tgt, idx, src, axis=0):
    return tgt.clone().index_add_(axis, idx, src)
def _segment_sum(src, seg, n):
    out = torch.zeros((n,) + src.shape[1:], dtype=src.dtype)
    return out.index_add_(0, seg, src)
def _pad(x, pw):
    flat = tuple(v for pair in reversed(pw) for v in pair)
    return torch.nn.functional.pad(x, flat)

kernel_name = 'concat_heads__linear_attn_block__ssm_conv_s4d__prompt'

def setup_inputs():

    _w = (lambda a: (lambda t: t.to(torch.bfloat16) if t.is_floating_point() else t)(torch.as_tensor(a)))
    def _A_setup():
        B, S, D = 1, 2048, 640
        return {
            "x": _w((lambda *s: torch.randn(*s, dtype=torch.bfloat16) * 0.05)(B, S, D)),
            "wqkv": _w((lambda *s: torch.randn(*s, dtype=torch.bfloat16) * 0.4)(D, 3 * D)),
            "wo": _w((lambda *s: torch.randn(*s, dtype=torch.bfloat16) * 0.05)(D, D)),
        }
    _A_in = _A_setup()

    def _B_setup():
        Bsz, L, D, N = 1, 2048, 640, 192
        return {
            "u": _w((lambda *s: torch.randn(*s, dtype=torch.bfloat16) * 0.05)(Bsz, L, D)),
            "log_A_real": _w((lambda *s: torch.randn(*s, dtype=torch.bfloat16) * 6.4)(D, N) * 0.5),
            "A_imag": _w((lambda *s: torch.randn(*s, dtype=torch.bfloat16) * 0.05)(D, N) * 3.14),
            "C_real": _w((lambda *s: torch.randn(*s, dtype=torch.bfloat16) * 0.1)(D, N) / N ** 0.5),
            "C_imag": _w((lambda *s: torch.randn(*s, dtype=torch.bfloat16) * 0.1)(D, N) / N ** 0.5),
            "log_dt": _w((lambda *s: torch.randn(*s, dtype=torch.bfloat16) * 0.05)(D) * 0.5 - 4.0),
            "D_skip": _w((lambda *s: torch.randn(*s, dtype=torch.bfloat16) * 0.05)(D)),
        }
    _B_in = _B_setup()

    return {
        "A_x": _A_in["x"],
        "A_wqkv": _A_in["wqkv"],
        "A_wo": _A_in["wo"],
        "B_u": _B_in["u"],
        "B_log_A_real": _B_in["log_A_real"],
        "B_A_imag": _B_in["A_imag"],
        "B_C_real": _B_in["C_real"],
        "B_C_imag": _B_in["C_imag"],
        "B_log_dt": _B_in["log_dt"],
        "B_D_skip": _B_in["D_skip"],
    }

def reference(A_x, A_wqkv, A_wo, B_u, B_log_A_real, B_A_imag, B_C_real, B_C_imag, B_log_dt, B_D_skip):
    def _A_ref(x, wqkv, wo):
        B, S, D = x.shape
        H = 32; Dh = D // H
        qkv = (lambda x, *d: x.permute(*d))((x @ wqkv).reshape(B, S, 3, H, Dh), 2, 0, 3, 1, 4)
        q, k, v = qkv[0], qkv[1], qkv[2]
        phi = lambda t: torch.where(t > 0, t + 1, torch.exp(t)) + 1e-6
        pq, pk = phi(q), phi(k)
        kv = torch.einsum("bhsd,bhse->bhde", pk, v)
        z = torch.einsum("bhsd->bhd", pk)
        num = torch.einsum("bhqd,bhde->bhqe", pq, kv)
        den = torch.einsum("bhqd,bhd->bhq", pq, z)[..., None] + 1e-6
        o = num / den
        return (lambda x, *d: x.permute(*d))(o, 0, 2, 1, 3).reshape(B, S, D) @ wo

    def _B_ref(u, log_A_real, A_imag, C_real, C_imag, log_dt, D_skip):
        L = u.shape[1]
        dt = torch.exp(log_dt)
        A = -torch.exp(log_A_real) + 1j * A_imag
        C = (C_real + 1j * C_imag) * dt[:, None]
        pos = (lambda x, dt: x.to(dt))(torch.arange(L), log_dt.dtype)
        P = torch.exp((A * dt[:, None])[:, :, None] * pos[None, None, :])
        K = 2.0 * torch.einsum("dn,dnl->dl", C, P).real
        L2 = 2 * L
        u_t = (lambda x, *d: x.permute(*d))(u, 0, 2, 1)
        Kf = (lambda x, *a, **k: torch.fft.rfft(x.float(), *a, **k))(K, L2)
        Uf = (lambda x, *a, **k: torch.fft.rfft(x.float(), *a, **k))(u_t, L2)
        y = (lambda x, *a, **k: torch.fft.irfft(x, *a, **k).to(torch.bfloat16))(Uf * Kf[None, :, :], L2)[:, :, :L]
        y = (lambda x, *d: x.permute(*d))(y, 0, 2, 1)
        return y + u * D_skip[None, None, :]

    a = _A_ref(A_x, A_wqkv, A_wo)
    b = _B_ref(B_u, B_log_A_real, B_A_imag, B_C_real, B_C_imag, B_log_dt, B_D_skip)
    return torch.concatenate([a, b], axis=-1)
```

```cpp
#include <hip/hip_runtime.h>
#include <hip/hip_bf16.h>
#include <stdint.h>

#define DI __device__ __forceinline__
#define NOPK __attribute__((target("no-packed-fp32-ops")))
#define LDS_AS __attribute__((address_space(3)))
#define GLB_AS __attribute__((address_space(1)))

typedef __attribute__((ext_vector_type(8))) short s16x8;
typedef __attribute__((ext_vector_type(4))) short s16x4;
typedef __attribute__((ext_vector_type(8))) _Float16 f16x8;
typedef __attribute__((ext_vector_type(2))) _Float16 f16x2;
typedef __attribute__((ext_vector_type(8))) __bf16 b16x8;
typedef __attribute__((ext_vector_type(4))) __bf16 b16x4;
typedef __attribute__((ext_vector_type(2))) __bf16 b16x2;
typedef __attribute__((ext_vector_type(4))) float f32x4;
typedef __attribute__((ext_vector_type(2))) float f32x2;
typedef __attribute__((ext_vector_type(16))) float f32x16;
typedef __attribute__((ext_vector_type(4))) unsigned u32x4;
typedef __attribute__((ext_vector_type(2))) unsigned u32x2;

constexpr int SEQ = 2048, DM = 640, NMODE = 192;

constexpr size_t WS_FLAG = 0;
constexpr size_t WS_KVP  = 4096;
constexpr size_t WS_KVR  = WS_KVP + (size_t)8 * 32 * 1680 * 4;
constexpr size_t WS_K16  = WS_KVR + 8 * 4096;
constexpr size_t WS_KSC  = WS_K16 + (size_t)DM * SEQ * 2;
constexpr size_t WS_UT   = WS_KSC + 4096;
constexpr size_t WS_O    = WS_UT + (size_t)DM * SEQ * 2;
constexpr size_t WS_YT   = WS_O + (size_t)DM * SEQ * 2;

DI float bf2f(unsigned short h) { return __uint_as_float((unsigned)h << 16); }
DI unsigned pk_bf16(float a, float b) { f32x2 v = {a, b}; return __builtin_bit_cast(unsigned, __builtin_convertvector(v, b16x2)); }
DI unsigned pk_f16(float a, float b) { f32x2 v = {a, b}; return __builtin_bit_cast(unsigned, __builtin_convertvector(v, f16x2)); }
DI float bfr(float x) { return __uint_as_float(pk_bf16(x, 0.f) << 16); }
DI float lo16(unsigned p) { return __uint_as_float(p << 16); }
DI float hi16(unsigned p) { return __uint_as_float(p & 0xffff0000u); }

DI f32x4 mfma16(s16x8 a, s16x8 b, f32x4 c) {
  return __builtin_amdgcn_mfma_f32_16x16x32_bf16(__builtin_bit_cast(b16x8, a), __builtin_bit_cast(b16x8, b), c, 0, 0, 0);
}
DI f32x4 mfma16h(s16x8 a, s16x8 b, f32x4 c) {
  return __builtin_amdgcn_mfma_f32_16x16x32_f16(__builtin_bit_cast(f16x8, a), __builtin_bit_cast(f16x8, b), c, 0, 0, 0);
}
DI f32x16 mfma32h(s16x8 a, s16x8 b, f32x16 c) {
  return __builtin_amdgcn_mfma_f32_32x32x16_f16(__builtin_bit_cast(f16x8, a), __builtin_bit_cast(f16x8, b), c, 0, 0, 0);
}
DI s16x4 tr_read(const char* smem, unsigned off) {
  return __builtin_amdgcn_ds_read_tr16_b64_v4i16((LDS_AS s16x4*)(smem + off));
}
DI s16x8 cat8(s16x4 a, s16x4 b) { return __builtin_shufflevector(a, b, 0, 1, 2, 3, 4, 5, 6, 7); }

DI void bsync() {
  __builtin_amdgcn_fence(__ATOMIC_RELEASE, "workgroup");
  __builtin_amdgcn_s_barrier();
  __builtin_amdgcn_fence(__ATOMIC_ACQUIRE, "workgroup");
}
DI unsigned opq(unsigned x) { asm("" : "+v"(x)); return x; }
#define WAIT_V(n) asm volatile("s_waitcnt vmcnt(" #n ")" ::: "memory")
#define WAIT_L0() asm volatile("s_waitcnt lgkmcnt(0)" ::: "memory")
#define RAW_BAR() __builtin_amdgcn_s_barrier()

DI float phi(float t) {
  float a = bfr(t + 1.0f);
  float e = bfr(__expf(t));
  float s = t > 0.f ? a : e;
  return bfr(s + 1e-6f);
}

constexpr int SLOT = 20480;

template <int NH, bool TRANS>
DI void gemm_core(const char* smem, const unsigned short* __restrict__ X, int ldx, int row0,
                  const unsigned short* __restrict__ W, int ldw, int colA, int colB,
                  f32x4 (&acc)[NH * 5]) {
  const int tid = threadIdx.x, w = tid >> 6, lane = tid & 63, r16 = lane & 15, h = lane >> 4;
  constexpr int NST = NH == 2 ? 10 : 5;
  unsigned srcoff[5];
#pragma unroll
  for (int i = 0; i < 5; ++i) {
    int c = (w + 4 * i) * 64 + lane;
    int unit = c / 640, cu = c - unit * 640;
    int pos = cu / 10, ch = cu - pos * 10;
    int k = pos ^ (((pos >> 3) & 1) << 2);
    int col, kk;
    if (NH == 2) { col = (unit ? colB : colA) + ch * 8; kk = k; }
    else         { col = colA + ch * 8; kk = unit * 64 + k; }
    srcoff[i] = (unsigned)(kk * ldw + col) * 2u;
  }
  const char* Wb = (const char*)W;
  const unsigned stage_stride = (unsigned)(NH == 2 ? 64 : 128) * ldw * 2u;
  auto issue = [&](int st) __attribute__((always_inline)) {
    const char* base = Wb + (size_t)st * stage_stride;
    char* dst = (char*)smem + (st % 3) * SLOT + w * 1024;
#pragma unroll
    for (int i = 0; i < 5; ++i)
      __builtin_amdgcn_global_load_lds((const GLB_AS unsigned*)(base + srcoff[i]), (LDS_AS unsigned*)(dst + i * 4096), 16, 0, 0);
  };
  const unsigned short* xp = X + (size_t)(row0 + 16 * w + r16) * ldx + 8 * h;
  s16x8 xa[20];
  issue(0);
#pragma unroll
  for (int ks = 0; ks < 2 * (NH == 2 ? 1 : 2); ++ks)
    asm volatile("global_load_dwordx4 %0, %1, off offset:%2" : "=v"(xa[ks]) : "v"(xp), "i"(ks * 64) : "memory");
  issue(1);
#pragma unroll
  for (int ks = 2 * (NH == 2 ? 1 : 2); ks < 20; ++ks)
    asm volatile("global_load_dwordx4 %0, %1, off offset:%2" : "=v"(xa[ks]) : "v"(xp), "i"(ks * 64) : "memory");
#pragma unroll
  for (int t = 0; t < NH * 5; ++t) acc[t] = f32x4{0.f, 0.f, 0.f, 0.f};
  const int q = r16 >> 2, p = lane & 3;
  unsigned trb[2];
#pragma unroll
  for (int t = 0; t < 2; ++t) {
    int k = 8 * h + 4 * t + q;
    int pos = k ^ (((k >> 3) & 1) << 2);
    trb[t] = pos * 160 + p * 8;
  }
  constexpr int KPS = NH == 2 ? 2 : 4;
#pragma unroll
  for (int st = 0; st < NST; ++st) {
    if (st == 0) { if (NH == 2) WAIT_V(23); else WAIT_V(21); }
    else if (st == 1) { if (NH == 2) WAIT_V(21); else WAIT_V(17); }
    else if (st == NST - 1) WAIT_V(0);
    else WAIT_V(5);
    WAIT_L0();
    RAW_BAR();
    if (st + 2 < NST) issue(st + 2);
#pragma unroll
    for (int i = 0; i < KPS; ++i) asm volatile("" : "+v"(xa[st * KPS + i]));
    const unsigned sb = (st % 3) * SLOT;
#pragma unroll
    for (int kk = 0; kk < KPS; ++kk) {
      const int ks = st * KPS + kk;
#pragma unroll
      for (int t = 0; t < NH * 5; ++t) {
        const int unit = NH == 2 ? t / 5 : kk >> 1;
        const int k32 = NH == 2 ? kk : (kk & 1);
        const int c = NH == 2 ? t % 5 : t;
        const unsigned o = sb + unit * 10240 + k32 * 32 * 160 + c * 32;
        s16x8 wf = cat8(tr_read(smem, o + trb[0]), tr_read(smem, o + trb[1]));
        if (TRANS) acc[t] = mfma16(wf, xa[ks], acc[t]);
        else       acc[t] = mfma16(xa[ks], wf, acc[t]);
      }
    }
  }
}

DI void transpose_tile(char* smem, const unsigned short* __restrict__ src, int lds_, int r0, int c0,
                       unsigned short* __restrict__ dst, int ldd) {
  unsigned short* t = (unsigned short*)smem;
  const int tid = threadIdx.x;
#pragma unroll
  for (int i = 0; i < 2; ++i) {
    int idx = tid + 256 * i, r = idx >> 3, c8 = idx & 7;
    u32x4 v = *(const u32x4*)(src + (size_t)(r0 + r) * lds_ + c0 + c8 * 8);
#pragma unroll
    for (int j = 0; j < 4; ++j) *(unsigned*)(t + r * 66 + c8 * 8 + 2 * j) = v[j];
  }
  bsync();
#pragma unroll
  for (int i = 0; i < 2; ++i) {
    int idx = tid + 256 * i, c = idx >> 3, r8 = idx & 7;
    u32x4 v;
#pragma unroll
    for (int j = 0; j < 4; ++j)
      v[j] = (unsigned)t[(r8 * 8 + 2 * j) * 66 + c] | ((unsigned)t[(r8 * 8 + 2 * j + 1) * 66 + c] << 16);
    *(u32x4*)(dst + (size_t)(c0 + c) * ldd + r0 + r8 * 8) = v;
  }
}

struct cplx { float r, i; };
DI cplx cmul(cplx a, cplx b) { return cplx{a.r * b.r - a.i * b.i, a.r * b.i + a.i * b.r}; }
DI cplx cexp_at(float ar, float ai, float p) {
  float m = __expf(ar * p);
  float x = ai * p;
  float k = rintf(x * 0.15915494309189535f);
  float r = fmaf(-k, 6.2831854820251465f, x);
  r = fmaf(-k, -1.7484556000744487e-07f, r);
  float rv = r * 0.15915494309189535f;
  return cplx{m * __builtin_amdgcn_cosf(rv), m * __builtin_amdgcn_sinf(rv)};
}
constexpr int KG_PITCH = 800;
constexpr int KG_B = 48 * KG_PITCH;
constexpr int KG_T = 64 * KG_PITCH;
constexpr int KG_RED = KG_T + 1536;

DI void kgen_channel(char* smem, int d, const unsigned short* __restrict__ lar_, const unsigned short* __restrict__ aim_,
                     const unsigned short* __restrict__ cr_, const unsigned short* __restrict__ ci_,
                     const unsigned short* __restrict__ ldt_, unsigned short* __restrict__ K16, float* __restrict__ ksc) {
  const int tid = threadIdx.x, w = tid >> 6, lane = tid & 63;
  float* red = (float*)(smem + KG_RED);
  const int n = tid < NMODE ? tid : 0;
  const float dt = bfr(expf(bf2f(ldt_[d])));
  const float a = bfr(expf(bf2f(lar_[d * NMODE + n])));
  const float ar = -a * dt, ai = bf2f(aim_[d * NMODE + n]) * dt;
  cplx c = cplx{bf2f(cr_[d * NMODE + n]) * dt, bf2f(ci_[d * NMODE + n]) * dt};
  float s = tid < NMODE ? fabsf(c.r) + fabsf(c.i) : 0.f;
#pragma unroll
  for (int o = 32; o >= 1; o >>= 1) s += __shfl_xor(s, o);
  if (lane == 0) red[w] = s;
  bsync();
  const float S = 2.f * (red[0] + red[1] + red[2]) ;
  int es = (int)((__float_as_uint(S) >> 23) & 0xff);
  int se = 127 + 15 - (es - 126); se = se < 1 ? 1 : (se > 253 ? 253 : se);
  const float sc = __uint_as_float((unsigned)se << 23);
  if (tid == 0) ksc[d] = __uint_as_float((unsigned)(254 - se) << 23);
  if (tid < NMODE) {
    c.r *= sc; c.i *= sc;
    char* Ab = smem + 4 * n;
    char* Bb = smem + KG_B + 4 * n;
    auto putA = [&](int row, cplx v) __attribute__((always_inline)) { *(unsigned*)(Ab + row * KG_PITCH) = pk_f16(v.r, -v.i); };
    auto putB = [&](int row, cplx v) __attribute__((always_inline)) { *(unsigned*)(Bb + row * KG_PITCH) = pk_f16(v.r, v.i); };
    cplx e[8], f[16], g[8], cg[17];
    e[0] = cplx{1.f, 0.f}; e[1] = cexp_at(ar, ai, 1.f);
    e[2] = cmul(e[1], e[1]); e[3] = cmul(e[2], e[1]); e[4] = cmul(e[2], e[2]);
    e[5] = cmul(e[4], e[1]); e[6] = cmul(e[4], e[2]); e[7] = cmul(e[4], e[3]);
    f[0] = cplx{1.f, 0.f}; f[1] = cexp_at(ar, ai, 8.f);
    f[2] = cmul(f[1], f[1]); f[3] = cmul(f[2], f[1]); f[4] = cmul(f[2], f[2]);
    f[5] = cmul(f[4], f[1]); f[6] = cmul(f[4], f[2]); f[7] = cmul(f[4], f[3]); f[8] = cmul(f[4], f[4]);
#pragma unroll
    for (int j = 1; j < 8; ++j) f[8 + j] = cmul(f[8], f[j]);
#pragma unroll
    for (int j = 0; j < 16; ++j) putB(j, f[j]);
    g[0] = cplx{1.f, 0.f}; g[1] = cexp_at(ar, ai, 128.f);
    g[2] = cmul(g[1], g[1]); g[3] = cmul(g[2], g[1]); g[4] = cmul(g[2], g[2]);
    g[5] = cmul(g[4], g[1]); g[6] = cmul(g[4], g[2]); g[7] = cmul(g[4], g[3]);
    cplx g8 = cexp_at(ar, ai, 1024.f);
    cg[0] = c; cg[8] = cmul(c, g8); cg[16] = cmul(cg[8], g8);
#pragma unroll
    for (int j = 1; j < 8; ++j) { cg[j] = cmul(c, g[j]); cg[8 + j] = cmul(cg[8], g[j]); }
#pragma unroll
    for (int i = 0; i < 16; ++i) putA(i, (i & 7) ? cmul(cg[i >> 3], e[i & 7]) : cg[i >> 3]);
#pragma unroll
    for (int i = 0; i < 8; ++i) putA(16 + i, (i & 3) ? cmul(cg[2 + (i >> 2)], e[2 * (i & 3)]) : cg[2 + (i >> 2)]);
#pragma unroll
    for (int i = 0; i < 8; ++i) putA(24 + i, (i & 1) ? cmul(cg[4 + (i >> 1)], e[4]) : cg[4 + (i >> 1)]);
#pragma unroll
    for (int i = 0; i < 8; ++i) putA(32 + i, cg[8 + i]);
    putA(40, cg[16]);
  }
  bsync();
  unsigned short* T = (unsigned short*)(smem + KG_T);
  if (w < 3) {
    const int r16 = lane & 15, h = lane >> 4;
    f32x4 acc = {0.f, 0.f, 0.f, 0.f};
    const char* ap = smem + (16 * w + r16) * KG_PITCH + h * 16;
    const char* bp = smem + KG_B + r16 * KG_PITCH + h * 16;
#pragma unroll
    for (int ks = 0; ks < 12; ++ks)
      acc = mfma16h(*(const s16x8*)(ap + ks * 64), *(const s16x8*)(bp + ks * 64), acc);
#pragma unroll
    for (int rg = 0; rg < 4; ++rg) {
      int i = 4 * h + rg, j = r16, idx;
      if (w == 0) idx = (i & 7) + 128 * (i >> 3) + 8 * j;
      else if (w == 1) idx = i < 8 ? 256 + (i & 3) + 64 * ((i >> 2) & 1) + 4 * j : 384 + (i & 1) + 32 * ((i >> 1) & 3) + 2 * j;
      else idx = i < 8 ? 512 + 16 * i + j : ((i == 8 && j == 0) ? 640 : -1);
      if (idx >= 0) T[idx] = (unsigned short)(pk_f16(2.f * acc[rg], 0.f) & 0xffff);
    }
  }
  bsync();
  {
    u32x4 v;
#pragma unroll
    for (int jj = 0; jj < 4; ++jj) {
      unsigned two[2];
#pragma unroll
      for (int b = 0; b < 2; ++b) {
        int t = 8 * tid + 2 * jj + b, idx;
        if (t < 256) idx = t;
        else if (t < 512) idx = 128 + ((t + ((t >> 1) & 1)) >> 1);
        else if (t < 1024) idx = 256 + ((t + 1 + ((t >> 2) & 1)) >> 2);
        else idx = 384 + ((t + 3 + ((t >> 3) & 1)) >> 3);
        two[b] = T[idx];
      }
      v[jj] = two[0] | (two[1] << 16);
    }
    *(u32x4*)(K16 + (size_t)d * SEQ + 8 * tid) = v;
  }
}

__global__ NOPK __launch_bounds__(256, 2) void k_l1(const unsigned short* __restrict__ x, const unsigned short* __restrict__ wqkv,
                                               const unsigned short* __restrict__ u, const unsigned short* __restrict__ lar,
                                               const unsigned short* __restrict__ aim, const unsigned short* __restrict__ cr,
                                               const unsigned short* __restrict__ ci, const unsigned short* __restrict__ ldt,
                                               char* __restrict__ ws) {
  extern __shared__ __attribute__((aligned(16))) char smem[];
  const int b = blockIdx.x, tid = threadIdx.x;
  if (b < 256) {
    const int xcd = b & 7, idx = b >> 3;
    const int g = (xcd & 1) * 4 + (idx & 3), rt = (xcd >> 1) * 8 + (idx >> 2);
    if (b == 0 && tid < 8) ((unsigned*)(ws + WS_FLAG))[tid * 64] = 0u;
    f32x4 acc[10];
    gemm_core<2, false>(smem, x, DM, rt * 64, wqkv, 3 * DM, DM + 80 * g, 2 * DM + 80 * g, acc);
    const int w = tid >> 6, lane = tid & 63, r16 = lane & 15, h = lane >> 4;
    bsync();
#pragma unroll
    for (int t = 0; t < 10; ++t) {
      float v0 = bfr(acc[t][0]), v1 = bfr(acc[t][1]), v2 = bfr(acc[t][2]), v3 = bfr(acc[t][3]);
      if (t < 5) { v0 = phi(v0); v1 = phi(v1); v2 = phi(v2); v3 = phi(v3); }
      u32x2 pk = {pk_bf16(v0, v1), pk_bf16(v2, v3)};
      *(u32x2*)(smem + (16 * t + r16) * 144 + (16 * w + 4 * h) * 2) = pk;
    }
    bsync();
    float* outp = (float*)(ws + WS_KVP) + (size_t)(g * 32 + rt) * 1680;
    const int jobs_k[18] = {0, 0, 1, 1, 1, 2, 2, 2, 3, 3, 3, 4, 4, 0, 1, 2, 3, 4};
    const int jobs_v[18] = {0, 1, 0, 1, 2, 1, 2, 3, 2, 3, 4, 3, 4, -1, -1, -1, -1, -1};
    s16x8 ones;
#pragma unroll
    for (int i = 0; i < 8; ++i) ones[i] = (short)0x3f80;
#pragma unroll
    for (int j = 0; j < 18; ++j) {
      if ((j & 3) == w) {
        const int kt = jobs_k[j], vt = jobs_v[j];
        f32x4 dacc = {0.f, 0.f, 0.f, 0.f};
#pragma unroll
        for (int ks = 0; ks < 2; ++ks) {
          s16x8 af = *(const s16x8*)(smem + (16 * kt + r16) * 144 + ks * 64 + h * 16);
          s16x8 bf = vt >= 0 ? *(const s16x8*)(smem + (80 + 16 * vt + r16) * 144 + ks * 64 + h * 16) : ones;
          dacc = mfma16(af, bf, dacc);
        }
        const int kf = 16 * kt + 4 * h, hk = kf / 20, dd = kf - 20 * hk;
        if (vt >= 0) {
          const int vf = 16 * vt + r16, hv = vf / 20, e = vf - 20 * hv;
          if (hk == hv) *(f32x4*)(outp + (hk * 21 + e) * 20 + dd) = dacc;
        } else if (r16 == 0) {
          *(f32x4*)(outp + (hk * 21 + 20) * 20 + dd) = dacc;
        }
      }
    }
  } else if (b < 256 + 320) {
    const int t = b - 256;
    transpose_tile(smem, u, DM, (t / 10) * 64, (t % 10) * 64, (unsigned short*)(ws + WS_UT), SEQ);
  } else {
    kgen_channel(smem, b - 576, lar, aim, cr, ci, ldt, (unsigned short*)(ws + WS_K16), (float*)(ws + WS_KSC));
  }
}

constexpr int CV_KP = 4288;
constexpr int CV_U = 4 * CV_KP;
constexpr int CV_CH = CV_U + 7680;

DI void conv_pair(char* smem, int d0, const unsigned short* __restrict__ K16, const float* __restrict__ ksc,
                  const unsigned short* __restrict__ uT, unsigned short* __restrict__ yT) {
  const int tid = threadIdx.x, w = tid >> 6, lane = tid & 63;
  const int chn = w >> 1, par = w & 1, d = d0 + chn, t128 = tid & 127;
  char* cb = smem + chn * CV_CH;
  const unsigned short* kp = K16 + (size_t)d * SEQ;
#pragma unroll
  for (int i = 0; i < 2; ++i) {
    const int qc = t128 + 128 * i;
    u32x4 a = *(const u32x4*)(kp + 8 * qc);
    u32x2 bb = {0u, 0u};
    if (qc > 0) bb = *(const u32x2*)(kp + 8 * qc - 4);
    char* dst = cb + 64 + 16 * qc;
    unsigned s03 = __builtin_amdgcn_alignbit(a[0], bb[1], 16), s01 = __builtin_amdgcn_alignbit(a[1], a[0], 16),
             s12 = __builtin_amdgcn_alignbit(a[2], a[1], 16), s23 = __builtin_amdgcn_alignbit(a[3], a[2], 16),
             sb = __builtin_amdgcn_alignbit(bb[1], bb[0], 16);
    *(u32x4*)(dst) = a;
    *(u32x4*)(dst + CV_KP) = u32x4{s03, s01, s12, s23};
    *(u32x4*)(dst + 2 * CV_KP) = u32x4{opq(bb[1]), opq(a[0]), opq(a[1]), opq(a[2])};
    *(u32x4*)(dst + 3 * CV_KP) = u32x4{sb, s03, s01, s12};
  }
  if (t128 < 16) *(u32x4*)(cb + (t128 >> 2) * CV_KP + (t128 & 3) * 16) = u32x4{0u, 0u, 0u, 0u};
  const unsigned short* up = uT + (size_t)d * SEQ;
  u32x4 uv[4];
  unsigned mx = 0;
#pragma unroll
  for (int i = 0; i < 4; ++i) {
    uv[i] = *(const u32x4*)(up + 8 * (lane + 64 * (i ^ (par << 1))));
#pragma unroll
    for (int j = 0; j < 4; ++j) {
      unsigned lo = uv[i][j] & 0x7fffu, hi = (uv[i][j] >> 16) & 0x7fffu;
      mx = max(mx, max(lo, hi));
    }
  }
#pragma unroll
  for (int o = 32; o >= 1; o >>= 1) mx = max(mx, (unsigned)__shfl_xor((int)mx, o));
  int eu = (int)(mx >> 7);
  int se = 268 - eu; se = se > 253 ? 253 : (se < 1 ? 1 : se);
  const float usc = __uint_as_float((unsigned)se << 23);
  const float uinv = __uint_as_float((unsigned)(254 - se) << 23);
#pragma unroll
  for (int i = 0; i < 2; ++i) {
    const int ii = 2 * par + i;
    const int cidx = lane + 64 * ii;
    u32x4 src = uv[i];
    u32x4 o4;
#pragma unroll
    for (int j = 0; j < 4; ++j) o4[j] = pk_f16(lo16(src[j]) * usc, hi16(src[j]) * usc);
    *(u32x4*)(cb + CV_U + ((cidx >> 2) + 31) * 80 + (cidx & 3) * 16) = o4;
  }
  for (int i = t128; i < 155; i += 128) *(u32x4*)(cb + CV_U + i * 16) = u32x4{0u, 0u, 0u, 0u};
  bsync();
  const int i32 = lane & 31, h = lane >> 5, blk = (lane >> 4) & 1, q = (lane & 15) >> 2, p = lane & 3;
  const unsigned coff = chn * CV_CH;
  const unsigned ab = coff + q * CV_KP + 2 * (32 + 16 * blk - 8 * h + 4 * p) - 40;
  const unsigned ub = coff + CV_U + 80 * (i32 + 31) + 16 * h;
  f32x16 acc0 = {}, acc1 = {};
#pragma unroll 2
  for (int it = 0; it < 16; ++it) {
    const int m = 2 * it + par;
    const unsigned a0 = ab + 64 * m, u0 = ub - 80 * m;
#pragma unroll
    for (int ks = 0; ks < 2; ++ks) {
      const unsigned ao = a0 + 32 * (1 - ks);
      s16x8 A0 = cat8(tr_read(smem, ao + 8), tr_read(smem, ao));
      s16x8 A1 = cat8(tr_read(smem, ao + 2048 + 8), tr_read(smem, ao + 2048));
      s16x8 B0 = *(const s16x8*)(smem + u0 + 32 * ks);
      s16x8 B1 = *(const s16x8*)(smem + u0 + 2560 + 32 * ks);
      acc0 = mfma32h(A0, B0, acc0);
      acc1 = mfma32h(A0, B1, acc1);
      acc1 = mfma32h(A1, B0, acc1);
    }
  }
  bsync();
  float* rb = (float*)cb;
  if (par == 1) {
#pragma unroll
    for (int r = 0; r < 16; ++r) { rb[r * 64 + lane] = acc0[r]; rb[(16 + r) * 64 + lane] = acc1[r]; }
  }
  bsync();
  if (par == 0) {
    const float inv = ksc[d] * uinv;
    unsigned short* yp = yT + (size_t)d * SEQ;
#pragma unroll
    for (int c = 0; c < 2; ++c) {
#pragma unroll
      for (int g4 = 0; g4 < 4; ++g4) {
        float v[4];
#pragma unroll
        for (int j = 0; j < 4; ++j) {
          const int r = 4 * g4 + j;
          v[j] = ((c ? acc1[r] : acc0[r]) + rb[(16 * c + r) * 64 + lane]) * inv;
        }
        u32x2 pk = {pk_bf16(v[0], v[1]), pk_bf16(v[2], v[3])};
        *(u32x2*)(yp + 1024 * c + 32 * i32 + 8 * g4 + 4 * h) = pk;
      }
    }
  }
}

__global__ NOPK __launch_bounds__(256, 2) void k_l2(const unsigned short* __restrict__ x, const unsigned short* __restrict__ wqkv,
                                               char* __restrict__ ws) {
  extern __shared__ __attribute__((aligned(16))) char smem[];
  const int b = blockIdx.x, tid = threadIdx.x;
  if (b < 256) {
    const int xcd = b & 7, idx = b >> 3;
    const int g = (xcd & 1) * 4 + (idx & 3), rt = (xcd >> 1) * 8 + (idx >> 2);
    const int w = tid >> 6, lane = tid & 63, r16 = lane & 15, h = lane >> 4;
    unsigned* flag = (unsigned*)(ws + WS_FLAG) + g * 64;
    unsigned short* kvr = (unsigned short*)(ws + WS_KVR) + g * 2048;
    {
      float* red = (float*)(smem + 3 * SLOT);
      const float* pp = (const float*)(ws + WS_KVP) + (size_t)g * 32 * 1680;
#pragma unroll
      for (int i = 0; i < 2; ++i) {
        int id = tid + 256 * i;
        if (id < 448) {
          int p = id / 14, c = id - p * 14, cc = rt * 14 + c;
          f32x4 v = {0.f, 0.f, 0.f, 0.f};
          if (cc < 420) v = *(const f32x4*)(pp + (size_t)p * 1680 + cc * 4);
          *(f32x4*)(red + p * 56 + c * 4) = v;
        }
      }
      bsync();
      if (tid < 28) {
        float s0 = 0.f, s1 = 0.f;
#pragma unroll
        for (int p = 0; p < 32; ++p) { s0 += red[p * 56 + 2 * tid]; s1 += red[p * 56 + 2 * tid + 1]; }
        int e = rt * 56 + 2 * tid;
        if (e < 1680) __hip_atomic_store((unsigned*)(kvr + e), pk_bf16(s0, s1), __ATOMIC_RELAXED, __HIP_MEMORY_SCOPE_AGENT);
      }
      if (w == 0) {
        asm volatile("s_waitcnt vmcnt(0)" ::: "memory");
        if (lane == 0) __hip_atomic_fetch_add(flag, 1u, __ATOMIC_RELAXED, __HIP_MEMORY_SCOPE_AGENT);
      }
    }
    f32x4 acc[5];
    gemm_core<1, true>(smem, x, DM, rt * 64, wqkv, 3 * DM, 80 * g, 0, acc);
    s16x8 pqf[3];
    {
      unsigned pk[12];
#pragma unroll
      for (int t = 0; t < 5; ++t) {
        pk[2 * t] = pk_bf16(phi(bfr(acc[t][0])), phi(bfr(acc[t][1])));
        pk[2 * t + 1] = pk_bf16(phi(bfr(acc[t][2])), phi(bfr(acc[t][3])));
      }
      pk[10] = 0u; pk[11] = 0u;
#pragma unroll
      for (int j = 0; j < 3; ++j) pqf[j] = __builtin_bit_cast(s16x8, u32x4{pk[4 * j], pk[4 * j + 1], pk[4 * j + 2], pk[4 * j + 3]});
    }
    if (tid == 0) {
      unsigned spins = 0;
      while (__hip_atomic_load(flag, __ATOMIC_RELAXED, __HIP_MEMORY_SCOPE_AGENT) < 32u) {
        __builtin_amdgcn_s_sleep(1);
        if (++spins > (1u << 22)) break;
      }
    }
    bsync();
    {
      unsigned* tb = (unsigned*)smem;
      for (int i = tid; i < 840; i += 256)
        tb[i] = __hip_atomic_load((unsigned*)kvr + i, __ATOMIC_RELAXED, __HIP_MEMORY_SCOPE_AGENT);
      if (tid < 4) tb[840 + tid] = 0u;
    }
    bsync();
    f32x4 num[5], den = {0.f, 0.f, 0.f, 0.f};
#pragma unroll
    for (int t = 0; t < 5; ++t) num[t] = f32x4{0.f, 0.f, 0.f, 0.f};
    auto afrag = [&](int hrow, int erow, int j) __attribute__((always_inline)) -> s16x8 {
      s16x4 part[2];
#pragma unroll
      for (int hf = 0; hf < 2; ++hf) {
        int n0 = 32 * j + 16 * hf + 4 * h, hn = n0 / 20;
        int ad = (n0 < 80 && hn == hrow) ? ((hrow * 21 + erow) * 20 + (n0 - 20 * hn)) * 2 : 3360;
        part[hf] = *(const s16x4*)(smem + ad);
      }
      return cat8(part[0], part[1]);
    };
    const int jlo[5] = {0, 0, 0, 1, 1}, jhi[5] = {0, 1, 1, 2, 2};
#pragma unroll
    for (int mt = 0; mt < 5; ++mt) {
      const int e = 16 * mt + r16, he = e / 20, ee = e - 20 * he;
#pragma unroll
      for (int j = 0; j < 3; ++j)
        if (j >= jlo[mt] && j <= jhi[mt]) num[mt] = mfma16(afrag(he, ee, j), pqf[j], num[mt]);
    }
#pragma unroll
    for (int j = 0; j < 3; ++j) den = mfma16(afrag(r16 & 3, 20, j), pqf[j], den);
    float dn[4];
#pragma unroll
    for (int j = 0; j < 4; ++j) dn[j] = bfr(bfr(den[j]) + 1e-6f);
    bsync();
#pragma unroll
    for (int mt = 0; mt < 5; ++mt) {
      const int e0 = 16 * mt + 4 * h, hd = e0 / 20;
      const float dd = hd == 0 ? dn[0] : (hd == 1 ? dn[1] : (hd == 2 ? dn[2] : dn[3]));
      const float rc = 1.0f / dd;
      float o0 = bfr(num[mt][0]) * rc, o1 = bfr(num[mt][1]) * rc, o2 = bfr(num[mt][2]) * rc, o3 = bfr(num[mt][3]) * rc;
      *(u32x2*)(smem + 4096 + (16 * w + r16) * 176 + e0 * 2) = u32x2{pk_bf16(o0, o1), pk_bf16(o2, o3)};
    }
    bsync();
    unsigned short* op = (unsigned short*)(ws + WS_O);
    for (int i = tid; i < 640; i += 256) {
      int r = i / 10, c = i - r * 10;
      *(u32x4*)(op + (size_t)(rt * 64 + r) * DM + 80 * g + c * 8) = *(const u32x4*)(smem + 4096 + r * 176 + c * 16);
    }
  } else {
    conv_pair(smem, (b - 256) * 2, (const unsigned short*)(ws + WS_K16), (const float*)(ws + WS_KSC),
              (const unsigned short*)(ws + WS_UT), (unsigned short*)(ws + WS_YT));
  }
}

__global__ NOPK __launch_bounds__(256, 2) void k_l3(const unsigned short* __restrict__ wo, const unsigned short* __restrict__ u,
                                               const unsigned short* __restrict__ dsk, const char* __restrict__ ws,
                                               unsigned short* __restrict__ out) {
  extern __shared__ __attribute__((aligned(16))) char smem[];
  const int b = blockIdx.x, tid = threadIdx.x;
  if (b < 256) {
    const int xcd = b & 7, idx = b >> 3;
    const int g = (xcd & 1) * 4 + (idx & 3), rt = (xcd >> 1) * 8 + (idx >> 2);
    const int w = tid >> 6, lane = tid & 63, r16 = lane & 15, h = lane >> 4;
    f32x4 acc[5];
    gemm_core<1, true>(smem, (const unsigned short*)(ws + WS_O), DM, rt * 64, wo, DM, 80 * g, 0, acc);
    bsync();
#pragma unroll
    for (int t = 0; t < 5; ++t)
      *(u32x2*)(smem + (16 * w + r16) * 176 + (16 * t + 4 * h) * 2) = u32x2{pk_bf16(acc[t][0], acc[t][1]), pk_bf16(acc[t][2], acc[t][3])};
    bsync();
    for (int i = tid; i < 640; i += 256) {
      int r = i / 10, c = i - r * 10;
      *(u32x4*)(out + (size_t)(rt * 64 + r) * (2 * DM) + 80 * g + c * 8) = *(const u32x4*)(smem + r * 176 + c * 16);
    }
  } else {
    const int t = b - 256, l0 = (t / 10) * 64, d0 = (t % 10) * 64;
    const unsigned short* yT = (const unsigned short*)(ws + WS_YT);
    unsigned short* tl = (unsigned short*)smem;
#pragma unroll
    for (int i = 0; i < 2; ++i) {
      int idx = tid + 256 * i, r = idx >> 3, c8 = idx & 7;
      u32x4 v = *(const u32x4*)(yT + (size_t)(d0 + r) * SEQ + l0 + c8 * 8);
#pragma unroll
      for (int j = 0; j < 4; ++j) *(unsigned*)(tl + r * 66 + c8 * 8 + 2 * j) = v[j];
    }
    bsync();
#pragma unroll
    for (int i = 0; i < 2; ++i) {
      int idx = tid + 256 * i, l = idx >> 3, d8 = idx & 7;
      u32x4 uu = *(const u32x4*)(u + (size_t)(l0 + l) * DM + d0 + d8 * 8);
      u32x4 dk = *(const u32x4*)(dsk + d0 + d8 * 8);
      u32x4 v;
#pragma unroll
      for (int j = 0; j < 4; ++j) {
        float y0 = bf2f(tl[(d8 * 8 + 2 * j) * 66 + l]), y1 = bf2f(tl[(d8 * 8 + 2 * j + 1) * 66 + l]);
        float s0 = bfr(lo16(uu[j]) * lo16(dk[j])), s1 = bfr(hi16(uu[j]) * hi16(dk[j]));
        v[j] = pk_bf16(y0 + s0, y1 + s1);
      }
      *(u32x4*)(out + (size_t)(l0 + l) * (2 * DM) + DM + d0 + d8 * 8) = v;
    }
  }
}

extern "C" void kernel_launch(void* const* d_in, const int* in_sizes, int n_in, void* d_out, int out_size, void* d_ws,
                              size_t ws_size, hipStream_t stream) {
  const unsigned short* x = (const unsigned short*)d_in[0];
  const unsigned short* wqkv = (const unsigned short*)d_in[1];
  const unsigned short* wo = (const unsigned short*)d_in[2];
  const unsigned short* u = (const unsigned short*)d_in[3];
  const unsigned short* lar = (const unsigned short*)d_in[4];
  const unsigned short* aim = (const unsigned short*)d_in[5];
  const unsigned short* cr = (const unsigned short*)d_in[6];
  const unsigned short* ci = (const unsigned short*)d_in[7];
  const unsigned short* ldt = (const unsigned short*)d_in[8];
  const unsigned short* dsk = (const unsigned short*)d_in[9];
  char* ws = (char*)d_ws;
  constexpr int LDS1 = 3 * SLOT;
  constexpr int LDS2 = 3 * SLOT + 7168;
  constexpr int LDS3 = 3 * SLOT;
  (void)hipFuncSetAttribute((const void*)k_l1, hipFuncAttributeMaxDynamicSharedMemorySize, LDS1);
  (void)hipFuncSetAttribute((const void*)k_l2, hipFuncAttributeMaxDynamicSharedMemorySize, LDS2);
  (void)hipFuncSetAttribute((const void*)k_l3, hipFuncAttributeMaxDynamicSharedMemorySize, LDS3);
  k_l1<<<256 + 320 + 640, 256, LDS1, stream>>>(x, wqkv, u, lar, aim, cr, ci, ldt, ws);
  k_l2<<<256 + 320, 256, LDS2, stream>>>(x, wqkv, ws);
  k_l3<<<256 + 320, 256, LDS3, stream>>>(wo, u, dsk, ws, (unsigned short*)d_out);
}
```

```cpp
#include <hip/hip_runtime.h>
#include <hip/hip_bf16.h>
#include <stdint.h>

#define DI __device__ __forceinline__
#define NOPK __attribute__((target("no-packed-fp32-ops")))
#define LDS_AS __attribute__((address_space(3)))
#define GLB_AS __attribute__((address_space(1)))

typedef __attribute__((ext_vector_type(8))) short s16x8;
typedef __attribute__((ext_vector_type(4))) short s16x4;
typedef __attribute__((ext_vector_type(8))) _Float16 f16x8;
typedef __attribute__((ext_vector_type(2))) _Float16 f16x2;
typedef __attribute__((ext_vector_type(8))) __bf16 b16x8;
typedef __attribute__((ext_vector_type(4))) __bf16 b16x4;
typedef __attribute__((ext_vector_type(2))) __bf16 b16x2;
typedef __attribute__((ext_vector_type(4))) float f32x4;
typedef __attribute__((ext_vector_type(2))) float f32x2;
typedef __attribute__((ext_vector_type(16))) float f32x16;
typedef __attribute__((ext_vector_type(4))) unsigned u32x4;
typedef __attribute__((ext_vector_type(2))) unsigned u32x2;

#define L1_OFF 0
#define L1_N (256 + 160 + 160)
#define L2_OFF 0
#define L2_N (256 + 320)
#define L3_OFF 0
#define L3_N (256 + 320)
constexpr int SEQ = 2048, DM = 640, NMODE = 192;

constexpr size_t WS_FLAG = 0;
constexpr size_t WS_KVP  = 4096;
constexpr size_t WS_KVR  = WS_KVP + (size_t)8 * 32 * 1680 * 4;
constexpr size_t WS_K16  = WS_KVR + 8 * 840 * 8 + 64;
constexpr size_t WS_KSC  = WS_K16 + (size_t)DM * SEQ * 2;
constexpr size_t WS_UT   = WS_KSC + 4096;
constexpr size_t WS_O    = WS_UT + (size_t)DM * SEQ * 2;
constexpr size_t WS_YT   = WS_O + (size_t)DM * SEQ * 2;

#define STAMP(i) do { } while (0)
#define STAMP_DECL
#define STAMP_ARG
#define STAMP_PASS
#define STAMP_FLUSH() do { } while (0)
DI float bf2f(unsigned short h) { return __uint_as_float((unsigned)h << 16); }
DI unsigned pk_bf16(float a, float b) { f32x2 v = {a, b}; return __builtin_bit_cast(unsigned, __builtin_convertvector(v, b16x2)); }
DI unsigned pk_f16(float a, float b) { f32x2 v = {a, b}; return __builtin_bit_cast(unsigned, __builtin_convertvector(v, f16x2)); }
DI float bfr(float x) { return __uint_as_float(pk_bf16(x, 0.f) << 16); }
DI float lo16(unsigned p) { return __uint_as_float(p << 16); }
DI float hi16(unsigned p) { return __uint_as_float(p & 0xffff0000u); }

DI f32x4 mfma16(s16x8 a, s16x8 b, f32x4 c) {
  return __builtin_amdgcn_mfma_f32_16x16x32_bf16(__builtin_bit_cast(b16x8, a), __builtin_bit_cast(b16x8, b), c, 0, 0, 0);
}
DI f32x4 mfma16h(s16x8 a, s16x8 b, f32x4 c) {
  return __builtin_amdgcn_mfma_f32_16x16x32_f16(__builtin_bit_cast(f16x8, a), __builtin_bit_cast(f16x8, b), c, 0, 0, 0);
}
DI f32x16 mfma32h(s16x8 a, s16x8 b, f32x16 c) {
  return __builtin_amdgcn_mfma_f32_32x32x16_f16(__builtin_bit_cast(f16x8, a), __builtin_bit_cast(f16x8, b), c, 0, 0, 0);
}
DI s16x4 tr_read(const char* smem, unsigned off) {
  return __builtin_amdgcn_ds_read_tr16_b64_v4i16((LDS_AS s16x4*)(smem + off));
}
DI s16x8 cat8(s16x4 a, s16x4 b) { return __builtin_shufflevector(a, b, 0, 1, 2, 3, 4, 5, 6, 7); }

DI void bsync() {
  __builtin_amdgcn_fence(__ATOMIC_RELEASE, "workgroup");
  __builtin_amdgcn_s_barrier();
  __builtin_amdgcn_fence(__ATOMIC_ACQUIRE, "workgroup");
}
DI unsigned opq(unsigned x) { asm("" : "+v"(x)); return x; }
#define WAIT_V(n) asm volatile("s_waitcnt vmcnt(" #n ")" ::: "memory")
#define WAIT_L0() asm volatile("s_waitcnt lgkmcnt(0)" ::: "memory")
#define RAW_BAR() __builtin_amdgcn_s_barrier()

DI float phi(float t) {
  float a = bfr(t + 1.0f);
  float e = bfr(__expf(t));
  float s = t > 0.f ? a : e;
  return bfr(s + 1e-6f);
}

constexpr int SLOT = 20480;

template <int N> DI void wait_v() { asm volatile("s_waitcnt vmcnt(%0)" ::"n"(N) : "memory"); }

template <int NH, bool TRANS, class PRE>
DI void gemm_core(const char* smem, const unsigned short* __restrict__ X, int ldx, int row0,
                  const unsigned short* __restrict__ W, int ldw, int colA, int colB,
                  f32x4 (&acc)[NH * 5], PRE pre STAMP_ARG) {
  const int tid = threadIdx.x, w = tid >> 6, lane = tid & 63, r16 = lane & 15, h = lane >> 4;
  constexpr int NST = NH == 2 ? 10 : 5;
  constexpr int NSL = NH == 2 ? 4 : 3;
  constexpr int KPS = NH == 2 ? 2 : 4;
  STAMP(0);
  unsigned srcoff[5];
#pragma unroll
  for (int i = 0; i < 5; ++i) {
    int c = (w + 4 * i) * 64 + lane;
    int unit = c / 640, cu = c - unit * 640;
    int pos = cu / 10, ch = cu - pos * 10;
    int k = pos ^ (((pos >> 3) & 1) << 2);
    int col, kk;
    if (NH == 2) { col = (unit ? colB : colA) + ch * 8; kk = k; }
    else         { col = colA + ch * 8; kk = unit * 64 + k; }
    srcoff[i] = (unsigned)(kk * ldw + col) * 2u;
  }
  const char* Wb = (const char*)W;
  const unsigned stage_stride = (unsigned)(NH == 2 ? 64 : 128) * ldw * 2u;
  auto issue = [&](int st) __attribute__((always_inline)) {
    const char* base = Wb + (size_t)st * stage_stride;
    char* dst = (char*)smem + (st % NSL) * SLOT + w * 1024;
#pragma unroll
    for (int i = 0; i < 5; ++i)
      __builtin_amdgcn_global_load_lds((const GLB_AS unsigned*)(base + srcoff[i]), (LDS_AS unsigned*)(dst + i * 4096), 16, 0, 0);
  };
  const unsigned short* xp = X + (size_t)(row0 + 16 * w + r16) * ldx + 8 * h;
  s16x8 xa[20];
#pragma unroll
  for (int st = 0; st < NSL; ++st) {
    issue(st);
#pragma unroll
    for (int i = 0; i < KPS; ++i)
      asm volatile("global_load_dwordx4 %0, %1, off offset:%2" : "=v"(xa[st * KPS + i]) : "v"(xp), "i"((st * KPS + i) * 64) : "memory");
  }
#pragma unroll
  for (int ks = NSL * KPS; ks < 20; ++ks)
    asm volatile("global_load_dwordx4 %0, %1, off offset:%2" : "=v"(xa[ks]) : "v"(xp), "i"(ks * 64) : "memory");
  pre();
#pragma unroll
  for (int t = 0; t < NH * 5; ++t) acc[t] = f32x4{0.f, 0.f, 0.f, 0.f};
  const int q = r16 >> 2, p = lane & 3;
  unsigned trb[2];
#pragma unroll
  for (int t = 0; t < 2; ++t) {
    int k = 8 * h + 4 * t + q;
    int pos = k ^ (((k >> 3) & 1) << 2);
    trb[t] = pos * 160 + p * 8;
  }
  constexpr int TOT = NSL * 5 + 20;
#pragma unroll
  for (int st = 0; st < NST; ++st) {
    if (st < NSL) {
      constexpr int dummy = 0; (void)dummy;
      switch (st) {
        case 0: wait_v<TOT - (5 + KPS)>(); break;
        case 1: wait_v<TOT - 2 * (5 + KPS)>(); break;
        case 2: wait_v<TOT - 3 * (5 + KPS) + 5>(); break;
        default: wait_v<TOT - 4 * (5 + KPS) + 10>(); break;
      }
    } else {
      const int last = (st + NSL - 2) < (NST - 1) ? (st + NSL - 2) : (NST - 1);
      const int young = last - st;
      if (young == 0) wait_v<0>(); else if (young == 1) wait_v<5>(); else wait_v<10>();
    }
    WAIT_L0();
    RAW_BAR();
    STAMP(1 + st);
    if (st >= 1 && st + NSL - 1 < NST) issue(st + NSL - 1);
#pragma unroll
    for (int i = 0; i < KPS; ++i) asm volatile("" : "+v"(xa[st * KPS + i]));
    const unsigned sb = (st % NSL) * SLOT;
    s16x8 wf[KPS * NH * 5];
#pragma unroll
    for (int kk = 0; kk < KPS; ++kk)
#pragma unroll
      for (int t = 0; t < NH * 5; ++t) {
        const int unit = NH == 2 ? t / 5 : kk >> 1;
        const int k32 = NH == 2 ? kk : (kk & 1);
        const int c = NH == 2 ? t % 5 : t;
        const unsigned o = sb + unit * 10240 + k32 * 32 * 160 + c * 32;
        wf[kk * NH * 5 + t] = cat8(tr_read(smem, o + trb[0]), tr_read(smem, o + trb[1]));
      }
    __builtin_amdgcn_sched_barrier(0);
#pragma unroll
    for (int kk = 0; kk < KPS; ++kk)
#pragma unroll
      for (int t = 0; t < NH * 5; ++t) {
        const int ks = st * KPS + kk;
        if (TRANS) acc[t] = mfma16(wf[kk * NH * 5 + t], xa[ks], acc[t]);
        else       acc[t] = mfma16(xa[ks], wf[kk * NH * 5 + t], acc[t]);
      }
    __builtin_amdgcn_sched_barrier(0);
  }
  STAMP(12);
}
struct NoPre { DI void operator()() const {} };

DI void transpose_tile(char* smem, const unsigned short* __restrict__ src, int lds_, int r0, int c0,
                       unsigned short* __restrict__ dst, int ldd) {
  unsigned short* t = (unsigned short*)smem;
  const int tid = threadIdx.x;
#pragma unroll
  for (int i = 0; i < 2; ++i) {
    int idx = tid + 256 * i, r = idx >> 3, c8 = idx & 7;
    u32x4 v = *(const u32x4*)(src + (size_t)(r0 + r) * lds_ + c0 + c8 * 8);
#pragma unroll
    for (int j = 0; j < 4; ++j) *(unsigned*)(t + r * 66 + c8 * 8 + 2 * j) = v[j];
  }
  bsync();
#pragma unroll
  for (int i = 0; i < 2; ++i) {
    int idx = tid + 256 * i, c = idx >> 3, r8 = idx & 7;
    u32x4 v;
#pragma unroll
    for (int j = 0; j < 4; ++j)
      v[j] = (unsigned)t[(r8 * 8 + 2 * j) * 66 + c] | ((unsigned)t[(r8 * 8 + 2 * j + 1) * 66 + c] << 16);
    *(u32x4*)(dst + (size_t)(c0 + c) * ldd + r0 + r8 * 8) = v;
  }
}

struct cplx { float r, i; };
DI cplx cmul(cplx a, cplx b) { return cplx{a.r * b.r - a.i * b.i, a.r * b.i + a.i * b.r}; }
DI cplx cexp_at(float ar, float ai, float p) {
  float m = __expf(ar * p);
  float x = ai * p;
  float k = rintf(x * 0.15915494309189535f);
  float r = fmaf(-k, 6.2831854820251465f, x);
  r = fmaf(-k, -1.7484556000744487e-07f, r);
  float rv = r * 0.15915494309189535f;
  return cplx{m * __builtin_amdgcn_cosf(rv), m * __builtin_amdgcn_sinf(rv)};
}
constexpr int KG_PITCH = 288;
constexpr int KG_WAVE = 57 * KG_PITCH;

DI void kgen_wave(char* sm, int d, const unsigned short* __restrict__ lar_, const unsigned short* __restrict__ aim_,
                  const unsigned short* __restrict__ cr_, const unsigned short* __restrict__ ci_,
                  const unsigned short* __restrict__ ldt_, unsigned short* __restrict__ K16, float* __restrict__ ksc) {
  const int lane = threadIdx.x & 63, r16 = lane & 15, h = lane >> 4;
  float lar[3], aim[3], crv[3], civ[3];
#pragma unroll
  for (int c = 0; c < 3; ++c) {
    const int n = d * NMODE + 64 * c + lane;
    lar[c] = bf2f(lar_[n]); aim[c] = bf2f(aim_[n]); crv[c] = bf2f(cr_[n]); civ[c] = bf2f(ci_[n]);
  }
  const float dt = bfr(expf(bf2f(ldt_[d])));
  float s = 0.f;
#pragma unroll
  for (int c = 0; c < 3; ++c) { crv[c] *= dt; civ[c] *= dt; s += fabsf(crv[c]) + fabsf(civ[c]); }
#pragma unroll
  for (int o = 32; o >= 1; o >>= 1) s += __shfl_xor(s, o);
  const float S = 2.f * s;
  int es = (int)((__float_as_uint(S) >> 23) & 0xff);
  int se = 268 - es; se = se < 1 ? 1 : (se > 253 ? 253 : se);
  const float sc = __uint_as_float((unsigned)se << 23);
  if (lane == 0) ksc[d] = __uint_as_float((unsigned)(254 - se) << 23);
  f32x4 acc[3];
#pragma unroll
  for (int t = 0; t < 3; ++t) acc[t] = f32x4{0.f, 0.f, 0.f, 0.f};
  char* wp = sm + 4 * lane;
  const char* ap = sm + r16 * KG_PITCH + h * 16;
#pragma unroll 1
  for (int c = 0; c < 3; ++c) {
    const float larc = c == 0 ? lar[0] : (c == 1 ? lar[1] : lar[2]);
    const float aimc = c == 0 ? aim[0] : (c == 1 ? aim[1] : aim[2]);
    const float crc = c == 0 ? crv[0] : (c == 1 ? crv[1] : crv[2]);
    const float cic = c == 0 ? civ[0] : (c == 1 ? civ[1] : civ[2]);
    const float a = bfr(expf(larc));
    const float ar = -a * dt, ai = aimc * dt;
    const cplx cc = cplx{crc * sc, cic * sc};
    auto putA = [&](int row, cplx v) __attribute__((always_inline)) { *(unsigned*)(wp + row * KG_PITCH) = pk_f16(v.r, -v.i); };
    auto putB = [&](int row, cplx v) __attribute__((always_inline)) { *(unsigned*)(wp + (41 + row) * KG_PITCH) = pk_f16(v.r, v.i); };
    {
      cplx f[16];
      f[0] = cplx{1.f, 0.f}; f[1] = cexp_at(ar, ai, 8.f);
      f[2] = cmul(f[1], f[1]); f[3] = cmul(f[2], f[1]); f[4] = cmul(f[2], f[2]);
      f[5] = cmul(f[4], f[1]); f[6] = cmul(f[4], f[2]); f[7] = cmul(f[4], f[3]); f[8] = cmul(f[4], f[4]);
#pragma unroll
      for (int j = 1; j < 8; ++j) f[8 + j] = cmul(f[8], f[j]);
#pragma unroll
      for (int j = 0; j < 16; ++j) putB(j, f[j]);
    }
    cplx e[8], g[8], cg[17];
    e[0] = cplx{1.f, 0.f}; e[1] = cexp_at(ar, ai, 1.f);
    e[2] = cmul(e[1], e[1]); e[3] = cmul(e[2], e[1]); e[4] = cmul(e[2], e[2]);
    e[5] = cmul(e[4], e[1]); e[6] = cmul(e[4], e[2]); e[7] = cmul(e[4], e[3]);
    g[0] = cplx{1.f, 0.f}; g[1] = cexp_at(ar, ai, 128.f);
    g[2] = cmul(g[1], g[1]); g[3] = cmul(g[2], g[1]); g[4] = cmul(g[2], g[2]);
    g[5] = cmul(g[4], g[1]); g[6] = cmul(g[4], g[2]); g[7] = cmul(g[4], g[3]);
    const cplx g8 = cexp_at(ar, ai, 1024.f);
    cg[0] = cc; cg[8] = cmul(cc, g8); cg[16] = cmul(cg[8], g8);
#pragma unroll
    for (int j = 1; j < 8; ++j) { cg[j] = cmul(cc, g[j]); cg[8 + j] = cmul(cg[8], g[j]); }
#pragma unroll
    for (int i = 0; i < 16; ++i) putA(i, (i & 7) ? cmul(cg[i >> 3], e[i & 7]) : cg[i >> 3]);
#pragma unroll
    for (int i = 0; i < 8; ++i) putA(16 + i, (i & 3) ? cmul(cg[2 + (i >> 2)], e[2 * (i & 3)]) : cg[2 + (i >> 2)]);
#pragma unroll
    for (int i = 0; i < 8; ++i) putA(24 + i, (i & 1) ? cmul(cg[4 + (i >> 1)], e[4]) : cg[4 + (i >> 1)]);
#pragma unroll
    for (int i = 0; i < 8; ++i) putA(32 + i, cg[8 + i]);
    putA(40, cg[16]);
    s16x8 bf[4], af[4][3];
#pragma unroll
    for (int ks = 0; ks < 4; ++ks) {
      bf[ks] = *(const s16x8*)(ap + 41 * KG_PITCH + ks * 64);
#pragma unroll
      for (int t = 0; t < 3; ++t) af[ks][t] = *(const s16x8*)(ap + 16 * t * KG_PITCH + ks * 64);
    }
#pragma unroll
    for (int ks = 0; ks < 4; ++ks)
#pragma unroll
      for (int t = 0; t < 3; ++t) acc[t] = mfma16h(af[ks][t], bf[ks], acc[t]);
  }
  unsigned short* T = (unsigned short*)sm;
#pragma unroll
  for (int t = 0; t < 3; ++t)
#pragma unroll
    for (int rg = 0; rg < 4; ++rg) {
      const int i = 4 * h + rg, j = r16;
      int idx;
      if (t == 0) idx = (i & 7) + 128 * (i >> 3) + 8 * j;
      else if (t == 1) idx = i < 8 ? 256 + (i & 3) + 64 * ((i >> 2) & 1) + 4 * j : 384 + (i & 1) + 32 * ((i >> 1) & 3) + 2 * j;
      else idx = i < 8 ? 512 + 16 * i + j : ((i == 8 && j == 0) ? 640 : 700);
      T[idx] = (unsigned short)(pk_f16(2.f * acc[t][rg], 0.f) & 0xffff);
    }
#pragma unroll
  for (int ch = 0; ch < 4; ++ch) {
    const int t0 = 8 * (lane + 64 * ch);
    u32x4 v;
#pragma unroll
    for (int jj = 0; jj < 4; ++jj) {
      unsigned two[2];
#pragma unroll
      for (int b = 0; b < 2; ++b) {
        const int t = t0 + 2 * jj + b;
        int idx;
        if (ch == 0 && lane < 32) idx = t;
        else if (ch == 0) idx = 128 + ((t + ((t >> 1) & 1)) >> 1);
        else if (ch == 1) idx = 256 + ((t + 1 + ((t >> 2) & 1)) >> 2);
        else idx = 384 + ((t + 3 + ((t >> 3) & 1)) >> 3);
        two[b] = T[idx];
      }
      v[jj] = two[0] | (two[1] << 16);
    }
    *(u32x4*)(K16 + (size_t)d * SEQ + t0) = v;
  }
}

__global__ NOPK __launch_bounds__(256, 2) void k_l1(const unsigned short* __restrict__ x, const unsigned short* __restrict__ wqkv,
                                               const unsigned short* __restrict__ u, const unsigned short* __restrict__ lar,
                                               const unsigned short* __restrict__ aim, const unsigned short* __restrict__ cr,
                                               const unsigned short* __restrict__ ci, const unsigned short* __restrict__ ldt,
                                               char* __restrict__ ws) {
  extern __shared__ __attribute__((aligned(16))) char smem[];
  STAMP_DECL
  const int b = blockIdx.x + L1_OFF, tid = threadIdx.x;
  if (b < 256) {
    const int xcd = b & 7, idx = b >> 3;
    const int g = (xcd & 1) * 4 + (idx & 3), rt = (xcd >> 1) * 8 + (idx >> 2);
    if (tid < 27) {
      const int gi = b * 27 + tid;
      if (gi < 8 * 840) ((unsigned long long*)(ws + WS_KVR))[gi] = 0ull;
    }
    f32x4 acc[10];
    gemm_core<2, false>(smem, x, DM, rt * 64, wqkv, 3 * DM, DM + 80 * g, 2 * DM + 80 * g, acc, NoPre() STAMP_PASS);
    const int w = tid >> 6, lane = tid & 63, r16 = lane & 15, h = lane >> 4;
    bsync();
#pragma unroll
    for (int t = 0; t < 10; ++t) {
      float v0 = bfr(acc[t][0]), v1 = bfr(acc[t][1]), v2 = bfr(acc[t][2]), v3 = bfr(acc[t][3]);
      if (t < 5) { v0 = phi(v0); v1 = phi(v1); v2 = phi(v2); v3 = phi(v3); }
      u32x2 pk = {pk_bf16(v0, v1), pk_bf16(v2, v3)};
      *(u32x2*)(smem + (16 * t + r16) * 144 + (16 * w + 4 * h) * 2) = pk;
    }
    bsync();
    float* outp = (float*)(ws + WS_KVP) + (size_t)(g * 32 + rt) * 1680;
    const int jobs_k[18] = {0, 0, 1, 1, 1, 2, 2, 2, 3, 3, 3, 4, 4, 0, 1, 2, 3, 4};
    const int jobs_v[18] = {0, 1, 0, 1, 2, 1, 2, 3, 2, 3, 4, 3, 4, -1, -1, -1, -1, -1};
    s16x8 ones;
#pragma unroll
    for (int i = 0; i < 8; ++i) ones[i] = (short)0x3f80;
#pragma unroll
    for (int j = 0; j < 18; ++j) {
      if ((j & 3) == w) {
        const int kt = jobs_k[j], vt = jobs_v[j];
        f32x4 dacc = {0.f, 0.f, 0.f, 0.f};
#pragma unroll
        for (int ks = 0; ks < 2; ++ks) {
          s16x8 af = *(const s16x8*)(smem + (16 * kt + r16) * 144 + ks * 64 + h * 16);
          s16x8 bf = vt >= 0 ? *(const s16x8*)(smem + (80 + 16 * vt + r16) * 144 + ks * 64 + h * 16) : ones;
          dacc = mfma16(af, bf, dacc);
        }
        const int kf = 16 * kt + 4 * h, hk = kf / 20, dd = kf - 20 * hk;
        if (vt >= 0) {
          const int vf = 16 * vt + r16, hv = vf / 20, e = vf - 20 * hv;
          if (hk == hv) *(f32x4*)(outp + (hk * 21 + e) * 20 + dd) = dacc;
        } else if (r16 == 0) {
          *(f32x4*)(outp + (hk * 21 + 20) * 20 + dd) = dacc;
        }
      }
    }
    STAMP(13);
    STAMP_FLUSH();
  } else if (b >= 416) {
    const int w = tid >> 6;
    kgen_wave(smem + w * KG_WAVE, (b - 416) * 4 + w, lar, aim, cr, ci, ldt, (unsigned short*)(ws + WS_K16), (float*)(ws + WS_KSC));
  } else if (b >= 256) {
    for (int t = (b - 256) * 2; t < (b - 256) * 2 + 2; ++t) {
      transpose_tile(smem, u, DM, (t / 10) * 64, (t % 10) * 64, (unsigned short*)(ws + WS_UT), SEQ);
      bsync();
    }
  }
}

constexpr int CV_KP = 4288;
constexpr int CV_U = 4 * CV_KP;
constexpr int CV_CH = CV_U + 7680;

DI void conv_pair(char* smem, int d0, const unsigned short* __restrict__ K16, const float* __restrict__ ksc,
                  const unsigned short* __restrict__ uT, unsigned short* __restrict__ yT) {
  const int tid = threadIdx.x, w = tid >> 6, lane = tid & 63;
  const int chn = w >> 1, par = w & 1, d = d0 + chn, t128 = tid & 127;
  char* cb = smem + chn * CV_CH;
  const unsigned short* kp = K16 + (size_t)d * SEQ;
#pragma unroll
  for (int i = 0; i < 2; ++i) {
    const int qc = t128 + 128 * i;
    u32x4 a = *(const u32x4*)(kp + 8 * qc);
    u32x2 bb = {0u, 0u};
    if (qc > 0) bb = *(const u32x2*)(kp + 8 * qc - 4);
    char* dst = cb + 64 + 16 * qc;
    unsigned s03 = __builtin_amdgcn_alignbit(a[0], bb[1], 16), s01 = __builtin_amdgcn_alignbit(a[1], a[0], 16),
             s12 = __builtin_amdgcn_alignbit(a[2], a[1], 16), s23 = __builtin_amdgcn_alignbit(a[3], a[2], 16),
             sb = __builtin_amdgcn_alignbit(bb[1], bb[0], 16);
    *(u32x4*)(dst) = a;
    *(u32x4*)(dst + CV_KP) = u32x4{s03, s01, s12, s23};
    *(u32x4*)(dst + 2 * CV_KP) = u32x4{opq(bb[1]), opq(a[0]), opq(a[1]), opq(a[2])};
    *(u32x4*)(dst + 3 * CV_KP) = u32x4{sb, s03, s01, s12};
  }
  if (t128 < 16) *(u32x4*)(cb + (t128 >> 2) * CV_KP + (t128 & 3) * 16) = u32x4{0u, 0u, 0u, 0u};
  const unsigned short* up = uT + (size_t)d * SEQ;
  u32x4 uv[4];
  unsigned mx = 0;
#pragma unroll
  for (int i = 0; i < 4; ++i) {
    uv[i] = *(const u32x4*)(up + 8 * (lane + 64 * (i ^ (par << 1))));
#pragma unroll
    for (int j = 0; j < 4; ++j) {
      unsigned lo = uv[i][j] & 0x7fffu, hi = (uv[i][j] >> 16) & 0x7fffu;
      mx = max(mx, max(lo, hi));
    }
  }
#pragma unroll
  for (int o = 32; o >= 1; o >>= 1) mx = max(mx, (unsigned)__shfl_xor((int)mx, o));
  int eu = (int)(mx >> 7);
  int se = 268 - eu; se = se > 253 ? 253 : (se < 1 ? 1 : se);
  const float usc = __uint_as_float((unsigned)se << 23);
  const float uinv = __uint_as_float((unsigned)(254 - se) << 23);
#pragma unroll
  for (int i = 0; i < 2; ++i) {
    const int ii = 2 * par + i;
    const int cidx = lane + 64 * ii;
    u32x4 src = uv[i];
    u32x4 o4;
#pragma unroll
    for (int j = 0; j < 4; ++j) o4[j] = pk_f16(lo16(src[j]) * usc, hi16(src[j]) * usc);
    *(u32x4*)(cb + CV_U + ((cidx >> 2) + 31) * 80 + (cidx & 3) * 16) = o4;
  }
  for (int i = t128; i < 155; i += 128) *(u32x4*)(cb + CV_U + i * 16) = u32x4{0u, 0u, 0u, 0u};
  bsync();
  const int i32 = lane & 31, h = lane >> 5, blk = (lane >> 4) & 1, q = (lane & 15) >> 2, p = lane & 3;
  const unsigned coff = chn * CV_CH;
  const unsigned ab = coff + q * CV_KP + 2 * (32 + 16 * blk - 8 * h + 4 * p) - 40;
  const unsigned ub = coff + CV_U + 80 * (i32 + 31) + 16 * h;
  f32x16 acc0 = {}, acc1 = {};
  s16x8 fa[2][8];
  auto ldfr = [&](int it, s16x8 (&f)[8]) __attribute__((always_inline)) {
    const int m = 2 * it + par;
    const unsigned a0 = ab + 64 * m, u0 = ub - 80 * m;
#pragma unroll
    for (int ks = 0; ks < 2; ++ks) {
      const unsigned ao = a0 + 32 * (1 - ks);
      f[ks] = cat8(tr_read(smem, ao + 8), tr_read(smem, ao));
      f[4 + ks] = *(const s16x8*)(smem + u0 + 32 * ks);
      f[6 + ks] = *(const s16x8*)(smem + u0 + 2560 + 32 * ks);
      f[2 + ks] = cat8(tr_read(smem, ao + 2048 + 8), tr_read(smem, ao + 2048));
    }
  };
  auto mm = [&](s16x8 (&f)[8]) __attribute__((always_inline)) {
#pragma unroll
    for (int ks = 0; ks < 2; ++ks) {
      acc0 = mfma32h(f[ks], f[4 + ks], acc0);
      acc1 = mfma32h(f[ks], f[6 + ks], acc1);
      acc1 = mfma32h(f[2 + ks], f[4 + ks], acc1);
    }
  };
  ldfr(0, fa[0]);
#pragma unroll 1
  for (int it = 0; it < 16; it += 2) {
    ldfr(it + 1, fa[1]);
    __builtin_amdgcn_sched_barrier(0);
    mm(fa[0]);
    __builtin_amdgcn_sched_barrier(0);
    ldfr(it + 2 < 16 ? it + 2 : 0, fa[0]);
    __builtin_amdgcn_sched_barrier(0);
    mm(fa[1]);
    __builtin_amdgcn_sched_barrier(0);
  }
  bsync();
  float* rb = (float*)cb;
  if (par == 1) {
#pragma unroll
    for (int r = 0; r < 16; ++r) { rb[r * 64 + lane] = acc0[r]; rb[(16 + r) * 64 + lane] = acc1[r]; }
  }
  bsync();
  if (par == 0) {
    const float inv = ksc[d] * uinv;
    unsigned short* yp = yT + (size_t)d * SEQ;
#pragma unroll
    for (int c = 0; c < 2; ++c) {
#pragma unroll
      for (int g4 = 0; g4 < 4; ++g4) {
        float v[4];
#pragma unroll
        for (int j = 0; j < 4; ++j) {
          const int r = 4 * g4 + j;
          v[j] = ((c ? acc1[r] : acc0[r]) + rb[(16 * c + r) * 64 + lane]) * inv;
        }
        u32x2 pk = {pk_bf16(v[0], v[1]), pk_bf16(v[2], v[3])};
        *(u32x2*)(yp + 1024 * c + 32 * i32 + 8 * g4 + 4 * h) = pk;
      }
    }
  }
}

__global__ NOPK __launch_bounds__(256, 2) void k_l2(const unsigned short* __restrict__ x, const unsigned short* __restrict__ wqkv,
                                               char* __restrict__ ws) {
  extern __shared__ __attribute__((aligned(16))) char smem[];
  STAMP_DECL
  const int b = blockIdx.x + L2_OFF, tid = threadIdx.x;
  if (b < 256) {
    const int xcd = b & 7, idx = b >> 3;
    const int g = (xcd & 1) * 4 + (idx & 3), rt = (xcd >> 1) * 8 + (idx >> 2);
    const int w = tid >> 6, lane = tid & 63, r16 = lane & 15, h = lane >> 4;
    unsigned long long* gran = (unsigned long long*)(ws + WS_KVR) + g * 840;
    f32x4 pv[2];
    {
      const float* pp = (const float*)(ws + WS_KVP) + (size_t)g * 32 * 1680;
#pragma unroll
      for (int i = 0; i < 2; ++i) {
        int id = tid + 256 * i; id = id < 448 ? id : 447;
        int p = id / 14, c = id - p * 14, cc = rt * 14 + c; cc = cc < 420 ? cc : 419;
        const float* src = pp + (size_t)p * 1680 + cc * 4;
        asm volatile("global_load_dwordx4 %0, %1, off" : "=v"(pv[i]) : "v"(src) : "memory");
      }
    }
    auto pre = [&]() __attribute__((always_inline)) {
      float* red = (float*)(smem + 3 * SLOT);
      wait_v<35>();
      asm volatile("" : "+v"(pv[0]), "+v"(pv[1]));
#pragma unroll
      for (int i = 0; i < 2; ++i) {
        int id = tid + 256 * i;
        if (id < 448) { int p = id / 14, c = id - p * 14; *(f32x4*)(red + p * 56 + c * 4) = pv[i]; }
      }
      WAIT_L0();
      RAW_BAR();
      if (tid < 28) {
        float s0 = 0.f, s1 = 0.f;
#pragma unroll
        for (int p = 0; p < 32; ++p) { s0 += red[p * 56 + 2 * tid]; s1 += red[p * 56 + 2 * tid + 1]; }
        int e = rt * 28 + tid;
        if (e < 840) __hip_atomic_store(gran + e, (1ull << 32) | (unsigned long long)pk_bf16(s0, s1), __ATOMIC_RELAXED, __HIP_MEMORY_SCOPE_AGENT);
      }
    };
    f32x4 acc[5];
    gemm_core<1, true>(smem, x, DM, rt * 64, wqkv, 3 * DM, 80 * g, 0, acc, pre STAMP_PASS);
    s16x8 pqf[3];
    {
      unsigned pk[12];
#pragma unroll
      for (int t = 0; t < 5; ++t) {
        pk[2 * t] = pk_bf16(phi(bfr(acc[t][0])), phi(bfr(acc[t][1])));
        pk[2 * t + 1] = pk_bf16(phi(bfr(acc[t][2])), phi(bfr(acc[t][3])));
      }
      pk[10] = 0u; pk[11] = 0u;
#pragma unroll
      for (int j = 0; j < 3; ++j) pqf[j] = __builtin_bit_cast(s16x8, u32x4{pk[4 * j], pk[4 * j + 1], pk[4 * j + 2], pk[4 * j + 3]});
    }
    bsync();
    {
      unsigned* tb = (unsigned*)smem;
#pragma unroll
      for (int i = 0; i < 4; ++i) {
        const int gi = tid + 256 * i;
        if (gi < 840) {
          unsigned long long v; unsigned spins = 0;
          while (((v = __hip_atomic_load(gran + gi, __ATOMIC_RELAXED, __HIP_MEMORY_SCOPE_AGENT)) >> 32) != 1ull) {
            __builtin_amdgcn_s_sleep(1);
            if (++spins > (1u << 22)) break;
          }
          tb[gi] = (unsigned)v;
        }
      }
      if (tid < 4) tb[840 + tid] = 0u;
    }
    bsync();
    f32x4 num[5], den = {0.f, 0.f, 0.f, 0.f};
#pragma unroll
    for (int t = 0; t < 5; ++t) num[t] = f32x4{0.f, 0.f, 0.f, 0.f};
    auto afrag = [&](int hrow, int erow, int j) __attribute__((always_inline)) -> s16x8 {
      s16x4 part[2];
#pragma unroll
      for (int hf = 0; hf < 2; ++hf) {
        int n0 = 32 * j + 16 * hf + 4 * h, hn = n0 / 20;
        int ad = (n0 < 80 && hn == hrow) ? ((hrow * 21 + erow) * 20 + (n0 - 20 * hn)) * 2 : 3360;
        part[hf] = *(const s16x4*)(smem + ad);
      }
      return cat8(part[0], part[1]);
    };
    const int jlo[5] = {0, 0, 0, 1, 1}, jhi[5] = {0, 1, 1, 2, 2};
#pragma unroll
    for (int mt = 0; mt < 5; ++mt) {
      const int e = 16 * mt + r16, he = e / 20, ee = e - 20 * he;
#pragma unroll
      for (int j = 0; j < 3; ++j)
        if (j >= jlo[mt] && j <= jhi[mt]) num[mt] = mfma16(afrag(he, ee, j), pqf[j], num[mt]);
    }
#pragma unroll
    for (int j = 0; j < 3; ++j) den = mfma16(afrag(r16 & 3, 20, j), pqf[j], den);
    float dn[4];
#pragma unroll
    for (int j = 0; j < 4; ++j) dn[j] = bfr(bfr(den[j]) + 1e-6f);
    bsync();
#pragma unroll
    for (int mt = 0; mt < 5; ++mt) {
      const int e0 = 16 * mt + 4 * h, hd = e0 / 20;
      const float dd = hd == 0 ? dn[0] : (hd == 1 ? dn[1] : (hd == 2 ? dn[2] : dn[3]));
      const float rc = 1.0f / dd;
      float o0 = bfr(num[mt][0]) * rc, o1 = bfr(num[mt][1]) * rc, o2 = bfr(num[mt][2]) * rc, o3 = bfr(num[mt][3]) * rc;
      *(u32x2*)(smem + 4096 + (16 * w + r16) * 176 + e0 * 2) = u32x2{pk_bf16(o0, o1), pk_bf16(o2, o3)};
    }
    bsync();
    unsigned short* op = (unsigned short*)(ws + WS_O);
    for (int i = tid; i < 640; i += 256) {
      int r = i / 10, c = i - r * 10;
      *(u32x4*)(op + (size_t)(rt * 64 + r) * DM + 80 * g + c * 8) = *(const u32x4*)(smem + 4096 + r * 176 + c * 16);
    }
  } else {
    conv_pair(smem, (b - 256) * 2, (const unsigned short*)(ws + WS_K16), (const float*)(ws + WS_KSC),
              (const unsigned short*)(ws + WS_UT), (unsigned short*)(ws + WS_YT));
  }
}

__global__ NOPK __launch_bounds__(256, 2) void k_l3(const unsigned short* __restrict__ wo, const unsigned short* __restrict__ u,
                                               const unsigned short* __restrict__ dsk, const char* __restrict__ ws,
                                               unsigned short* __restrict__ out) {
  extern __shared__ __attribute__((aligned(16))) char smem[];
  STAMP_DECL
  const int b = blockIdx.x + L3_OFF, tid = threadIdx.x;
  if (b < 256) {
    const int xcd = b & 7, idx = b >> 3;
    const int g = (xcd & 1) * 4 + (idx & 3), rt = (xcd >> 1) * 8 + (idx >> 2);
    const int w = tid >> 6, lane = tid & 63, r16 = lane & 15, h = lane >> 4;
    f32x4 acc[5];
    gemm_core<1, true>(smem, (const unsigned short*)(ws + WS_O), DM, rt * 64, wo, DM, 80 * g, 0, acc, NoPre() STAMP_PASS);
    bsync();
#pragma unroll
    for (int t = 0; t < 5; ++t)
      *(u32x2*)(smem + (16 * w + r16) * 176 + (16 * t + 4 * h) * 2) = u32x2{pk_bf16(acc[t][0], acc[t][1]), pk_bf16(acc[t][2], acc[t][3])};
    bsync();
    for (int i = tid; i < 640; i += 256) {
      int r = i / 10, c = i - r * 10;
      *(u32x4*)(out + (size_t)(rt * 64 + r) * (2 * DM) + 80 * g + c * 8) = *(const u32x4*)(smem + r * 176 + c * 16);
    }
    STAMP(13);
    STAMP_FLUSH();
  } else {
    const int t = b - 256, l0 = (t / 10) * 64, d0 = (t % 10) * 64;
    const unsigned short* yT = (const unsigned short*)(ws + WS_YT);
    unsigned short* tl = (unsigned short*)smem;
#pragma unroll
    for (int i = 0; i < 2; ++i) {
      int idx = tid + 256 * i, r = idx >> 3, c8 = idx & 7;
      u32x4 v = *(const u32x4*)(yT + (size_t)(d0 + r) * SEQ + l0 + c8 * 8);
#pragma unroll
      for (int j = 0; j < 4; ++j) *(unsigned*)(tl + r * 66 + c8 * 8 + 2 * j) = v[j];
    }
    bsync();
#pragma unroll
    for (int i = 0; i < 2; ++i) {
      int idx = tid + 256 * i, l = idx >> 3, d8 = idx & 7;
      u32x4 uu = *(const u32x4*)(u + (size_t)(l0 + l) * DM + d0 + d8 * 8);
      u32x4 dk = *(const u32x4*)(dsk + d0 + d8 * 8);
      u32x4 v;
#pragma unroll
      for (int j = 0; j < 4; ++j) {
        float y0 = bf2f(tl[(d8 * 8 + 2 * j) * 66 + l]), y1 = bf2f(tl[(d8 * 8 + 2 * j + 1) * 66 + l]);
        float s0 = bfr(lo16(uu[j]) * lo16(dk[j])), s1 = bfr(hi16(uu[j]) * hi16(dk[j]));
        v[j] = pk_bf16(y0 + s0, y1 + s1);
      }
      *(u32x4*)(out + (size_t)(l0 + l) * (2 * DM) + DM + d0 + d8 * 8) = v;
    }
  }
}

extern "C" void kernel_launch(void* const* d_in, const int* in_sizes, int n_in, void* d_out, int out_size, void* d_ws,
                              size_t ws_size, hipStream_t stream) {
  const unsigned short* x = (const unsigned short*)d_in[0];
  const unsigned short* wqkv = (const unsigned short*)d_in[1];
  const unsigned short* wo = (const unsigned short*)d_in[2];
  const unsigned short* u = (const unsigned short*)d_in[3];
  const unsigned short* lar = (const unsigned short*)d_in[4];
  const unsigned short* aim = (const unsigned short*)d_in[5];
  const unsigned short* cr = (const unsigned short*)d_in[6];
  const unsigned short* ci = (const unsigned short*)d_in[7];
  const unsigned short* ldt = (const unsigned short*)d_in[8];
  const unsigned short* dsk = (const unsigned short*)d_in[9];
  char* ws = (char*)d_ws;
  constexpr int LDS1 = 4 * SLOT;
  constexpr int LDS2 = 3 * SLOT + 7168;
  constexpr int LDS3 = 3 * SLOT;
  (void)hipFuncSetAttribute((const void*)k_l1, hipFuncAttributeMaxDynamicSharedMemorySize, LDS1);
  (void)hipFuncSetAttribute((const void*)k_l2, hipFuncAttributeMaxDynamicSharedMemorySize, LDS2);
  (void)hipFuncSetAttribute((const void*)k_l3, hipFuncAttributeMaxDynamicSharedMemorySize, LDS3);
  k_l1<<<L1_N, 256, LDS1, stream>>>(x, wqkv, u, lar, aim, cr, ci, ldt, ws);
  k_l2<<<L2_N, 256, LDS2, stream>>>(x, wqkv, ws);
  k_l3<<<L3_N, 256, LDS3, stream>>>(wo, u, dsk, ws, (unsigned short*)d_out);
}
```

```cpp
#include <hip/hip_runtime.h>
#include <hip/hip_bf16.h>
#include <stdint.h>

#define DI __device__ __forceinline__
#define NOPK __attribute__((target("no-packed-fp32-ops")))
#define AINL __attribute__((always_inline))
#define LDS_AS __attribute__((address_space(3)))
#define GLB_AS __attribute__((address_space(1)))

typedef __attribute__((ext_vector_type(8))) short s16x8;
typedef __attribute__((ext_vector_type(4))) short s16x4;
typedef __attribute__((ext_vector_type(8))) _Float16 f16x8;
typedef __attribute__((ext_vector_type(2))) _Float16 f16x2;
typedef __attribute__((ext_vector_type(8))) __bf16 b16x8;
typedef __attribute__((ext_vector_type(2))) __bf16 b16x2;
typedef __attribute__((ext_vector_type(4))) float f32x4;
typedef __attribute__((ext_vector_type(2))) float f32x2;
typedef __attribute__((ext_vector_type(16))) float f32x16;
typedef __attribute__((ext_vector_type(4))) unsigned u32x4;
typedef __attribute__((ext_vector_type(2))) unsigned u32x2;
typedef unsigned long long u64;

#define DBG_NPASS 2
#define DBG_NCH 3
constexpr int SEQ = 2048, DM = 640, NMODE = 192;
constexpr int NB_A = 256, NB_R = 32, NB_B = 160;

constexpr size_t WS_EPOCH = 0;
constexpr size_t WS_F1  = 4096;
constexpr size_t WS_F2  = WS_F1 + 256 * 128;
constexpr size_t WS_F3  = WS_F2 + 32 * 128;
constexpr size_t WS_KVP = WS_F3 + 256 * 128;
constexpr size_t WS_KVR = WS_KVP + (size_t)8 * 32 * 1680 * 4;
constexpr size_t WS_O   = WS_KVR + 32 * 1024;

DI float bf2f(unsigned short h) { return __uint_as_float((unsigned)h << 16); }
DI unsigned pk_bf16(float a, float b) { f32x2 v = {a, b}; return __builtin_bit_cast(unsigned, __builtin_convertvector(v, b16x2)); }
DI unsigned pk_f16(float a, float b) { f32x2 v = {a, b}; return __builtin_bit_cast(unsigned, __builtin_convertvector(v, f16x2)); }
DI float bfr(float x) { return __uint_as_float(pk_bf16(x, 0.f) << 16); }
DI float lo16(unsigned p) { return __uint_as_float(p << 16); }
DI float hi16(unsigned p) { return __uint_as_float(p & 0xffff0000u); }
DI unsigned opq(unsigned x) { asm("" : "+v"(x)); return x; }

DI f32x4 mfma16(s16x8 a, s16x8 b, f32x4 c) {
  return __builtin_amdgcn_mfma_f32_16x16x32_bf16(__builtin_bit_cast(b16x8, a), __builtin_bit_cast(b16x8, b), c, 0, 0, 0);
}
DI f32x4 mfma16h(s16x8 a, s16x8 b, f32x4 c) {
  return __builtin_amdgcn_mfma_f32_16x16x32_f16(__builtin_bit_cast(f16x8, a), __builtin_bit_cast(f16x8, b), c, 0, 0, 0);
}
DI f32x16 mfma32h(s16x8 a, s16x8 b, f32x16 c) {
  return __builtin_amdgcn_mfma_f32_32x32x16_f16(__builtin_bit_cast(f16x8, a), __builtin_bit_cast(f16x8, b), c, 0, 0, 0);
}
DI s16x4 tr_read(const char* smem, unsigned off) {
  return __builtin_amdgcn_ds_read_tr16_b64_v4i16((LDS_AS s16x4*)(smem + off));
}
DI s16x8 cat8(s16x4 a, s16x4 b) { return __builtin_shufflevector(a, b, 0, 1, 2, 3, 4, 5, 6, 7); }

DI void bsync() {
  __builtin_amdgcn_fence(__ATOMIC_RELEASE, "workgroup");
  __builtin_amdgcn_s_barrier();
  __builtin_amdgcn_fence(__ATOMIC_ACQUIRE, "workgroup");
}
template <int N> DI void wait_v() { asm volatile("s_waitcnt vmcnt(%0)" ::"n"(N) : "memory"); }
DI void wait_l_bar() { asm volatile("s_waitcnt lgkmcnt(0)" ::: "memory"); __builtin_amdgcn_s_barrier(); }
DI void glds16(const void* g, char* l) {
  __builtin_amdgcn_global_load_lds((const GLB_AS unsigned*)g, (LDS_AS unsigned*)l, 16, 0, 0);
}
DI void st16_sc1(char* ws, size_t off, u32x4 v) {
  __builtin_amdgcn_raw_buffer_store_b128(v, __builtin_amdgcn_make_buffer_rsrc(ws, 0, 64u << 20, 0x00020000), (int)off, 0, 16);
}
DI void st4_sc1(void* p, unsigned v) { __hip_atomic_store((unsigned*)p, v, __ATOMIC_RELAXED, __HIP_MEMORY_SCOPE_AGENT); }
DI u64 flag_val(unsigned tag) { return ((u64)tag << 32) | (u64)(tag ^ 0x9e3779b9u); }
DI void flag_set(char* ws, size_t off, unsigned tag) {
  __hip_atomic_store((u64*)(ws + off), flag_val(tag), __ATOMIC_RELAXED, __HIP_MEMORY_SCOPE_AGENT);
}
DI void flags_wait(const char* ws, size_t off, int n, unsigned tag) {
  const int lane = threadIdx.x & 63;
  const u64 want = flag_val(tag);
  const u64* p = (const u64*)(ws + off + (size_t)(lane < n ? lane : 0) * 128);
  for (unsigned spins = 0; spins < (1u << 20); ++spins) {
    const u64 v = __hip_atomic_load(p, __ATOMIC_RELAXED, __HIP_MEMORY_SCOPE_AGENT);
    if (__all(v == want)) break;
    __builtin_amdgcn_s_sleep(2);
  }
}

DI float phi(float t) {
  float a = bfr(t + 1.0f);
  float e = bfr(__expf(t));
  float s = t > 0.f ? a : e;
  return bfr(s + 1e-6f);
}

constexpr int SLOT = 20480;
constexpr int XS = 3 * SLOT;

struct WSrc { unsigned off[5]; };
DI void wsrc_init(WSrc& s, int ldw, int colA, int colB, int kB) {
  const int w = threadIdx.x >> 6, lane = threadIdx.x & 63;
#pragma unroll
  for (int i = 0; i < 5; ++i) {
    int c = (w + 4 * i) * 64 + lane;
    int unit = c >= 640, cu = c - unit * 640;
    int pos = cu / 10, ch = cu - pos * 10;
    int k = pos ^ (((pos >> 3) & 1) << 2);
    s.off[i] = (unsigned)((k + (unit ? kB : 0)) * ldw + (unit ? colB : colA) + ch * 8) * 2u;
  }
}
DI void wissue(const WSrc& s, const char* Wst, char* slot) {
  char* dst = slot + (threadIdx.x >> 6) * 1024;
#pragma unroll
  for (int i = 0; i < 5; ++i) glds16(Wst + s.off[i], dst + i * 4096);
}

template <int NA>
DI void gemm80(char* smem, const WSrc& ws_, const char* Wb, unsigned stage_stride, s16x8 (&xa)[20], f32x4 (&acc)[5],
               const unsigned (&trb)[2]) {
#pragma unroll
  for (int t = 0; t < 5; ++t) acc[t] = f32x4{0.f, 0.f, 0.f, 0.f};
#pragma unroll
  for (int st = 0; st < 5; ++st) {
    if (NA == 0) {
      if (st == 0) wait_v<10>(); else if (st < 4) wait_v<5>(); else wait_v<0>();
    } else {
      if (st == 0) wait_v<16>(); else if (st == 1) wait_v<12>(); else if (st == 2) wait_v<13>();
      else if (st == 3) wait_v<5>(); else wait_v<0>();
    }
    wait_l_bar();
    if (st >= 1 && st + 2 < 5) wissue(ws_, Wb + (size_t)(st + 2) * stage_stride, smem + ((st + 2) % 3) * SLOT);
    if (NA) {
#pragma unroll
      for (int i = 0; i < 4; ++i) asm volatile("" : "+v"(xa[st * 4 + i]));
    }
    const unsigned sb = (st % 3) * SLOT;
    s16x8 wf[2][5];
#pragma unroll
    for (int t = 0; t < 5; ++t) wf[0][t] = cat8(tr_read(smem, sb + t * 32 + trb[0]), tr_read(smem, sb + t * 32 + trb[1]));
#pragma unroll
    for (int kk = 0; kk < 4; ++kk) {
      if (kk < 3) {
        const unsigned o = sb + ((kk + 1) >> 1) * 10240 + ((kk + 1) & 1) * 5120;
#pragma unroll
        for (int t = 0; t < 5; ++t) wf[(kk + 1) & 1][t] = cat8(tr_read(smem, o + t * 32 + trb[0]), tr_read(smem, o + t * 32 + trb[1]));
      }
#pragma unroll
      for (int t = 0; t < 5; ++t) acc[t] = mfma16(wf[kk & 1][t], xa[st * 4 + kk], acc[t]);
    }
  }
}

DI void role_A(char* smem, int b, const unsigned short* __restrict__ x, const unsigned short* __restrict__ wqkv,
               const unsigned short* __restrict__ wo, char* __restrict__ ws, unsigned short* __restrict__ out) {
  const int tid = threadIdx.x, w = tid >> 6, lane = tid & 63, r16 = lane & 15, h = lane >> 4;
  const int xcd = b & 7, idx = b >> 3;
  const int g = (xcd & 1) * 4 + (idx & 3), rt = (xcd >> 1) * 8 + (idx >> 2);
  const unsigned tag = __hip_atomic_load((const unsigned*)(ws + WS_EPOCH), __ATOMIC_RELAXED, __HIP_MEMORY_SCOPE_AGENT) + 1u;
  unsigned trb[2];
  {
    const int q = r16 >> 2, p = lane & 3;
#pragma unroll
    for (int t = 0; t < 2; ++t) { int k = 8 * h + 4 * t + q; trb[t] = (k ^ (((k >> 3) & 1) << 2)) * 160 + p * 8; }
  }
  s16x8 xa[20];
  {
    WSrc wsr; wsrc_init(wsr, 3 * DM, DM + 80 * g, 2 * DM + 80 * g, 0);
    const char* Wb = (const char*)wqkv;
    constexpr unsigned wstride = 64u * 3 * DM * 2;
    const char* xs[2];
#pragma unroll
    for (int i = 0; i < 2; ++i) {
      const int row = 8 * (w + 4 * i) + (lane >> 3);
      xs[i] = (const char*)x + (size_t)(rt * 64 + row) * (DM * 2) + (((lane & 7) ^ (row & 7)) << 4);
    }
    auto xissue = [&](int st) AINL {
      char* dst = smem + XS + (st & 1) * 8192 + w * 1024;
      glds16(xs[0] + st * 128, dst); glds16(xs[1] + st * 128, dst + 4096);
    };
    wissue(wsr, Wb, smem); xissue(0); wissue(wsr, Wb + wstride, smem + SLOT);
    f32x4 acc[10];
#pragma unroll
    for (int t = 0; t < 10; ++t) acc[t] = f32x4{0.f, 0.f, 0.f, 0.f};
    const unsigned xrd = XS + (16 * w + r16) * 128;
#pragma unroll
    for (int st = 0; st < 10; ++st) {
      if (st < 9) wait_v<5>(); else wait_v<0>();
      wait_l_bar();
      if (st + 1 < 10) xissue(st + 1);
      if (st + 2 < 10) wissue(wsr, Wb + (size_t)(st + 2) * wstride, smem + ((st + 2) % 3) * SLOT);
      const unsigned sb = (st % 3) * SLOT;
#pragma unroll
      for (int kk = 0; kk < 2; ++kk)
        xa[2 * st + kk] = *(const s16x8*)(smem + xrd + (st & 1) * 8192 + (((4 * kk + h) ^ (r16 & 7)) << 4));
      s16x8 wf[2][5];
#pragma unroll
      for (int t = 0; t < 5; ++t) wf[0][t] = cat8(tr_read(smem, sb + t * 32 + trb[0]), tr_read(smem, sb + t * 32 + trb[1]));
#pragma unroll
      for (int j = 0; j < 4; ++j) {
        if (j < 3) {
          const unsigned o = sb + ((j + 1) & 1) * 10240 + ((j + 1) >> 1) * 5120;
#pragma unroll
          for (int t = 0; t < 5; ++t) wf[(j + 1) & 1][t] = cat8(tr_read(smem, o + t * 32 + trb[0]), tr_read(smem, o + t * 32 + trb[1]));
        }
#pragma unroll
        for (int t = 0; t < 5; ++t) acc[(j & 1) * 5 + t] = mfma16(xa[2 * st + (j >> 1)], wf[j & 1][t], acc[(j & 1) * 5 + t]);
      }
    }
    bsync();
#pragma unroll
    for (int t = 0; t < 10; ++t) {
      float v0 = bfr(acc[t][0]), v1 = bfr(acc[t][1]), v2 = bfr(acc[t][2]), v3 = bfr(acc[t][3]);
      if (t < 5) { v0 = phi(v0); v1 = phi(v1); v2 = phi(v2); v3 = phi(v3); }
      *(u32x2*)(smem + (16 * t + r16) * 144 + (16 * w + 4 * h) * 2) = u32x2{pk_bf16(v0, v1), pk_bf16(v2, v3)};
    }
    bsync();
    const size_t outp = WS_KVP + (size_t)(g * 32 + rt) * 1680 * 4;
    const int jobs_k[18] = {0, 0, 1, 1, 1, 2, 2, 2, 3, 3, 3, 4, 4, 0, 1, 2, 3, 4};
    const int jobs_v[18] = {0, 1, 0, 1, 2, 1, 2, 3, 2, 3, 4, 3, 4, -1, -1, -1, -1, -1};
    s16x8 ones;
#pragma unroll
    for (int i = 0; i < 8; ++i) ones[i] = (short)0x3f80;
#pragma unroll
    for (int j = 0; j < 18; ++j) {
      if ((j & 3) == w) {
        const int kt = jobs_k[j], vt = jobs_v[j];
        f32x4 dacc = {0.f, 0.f, 0.f, 0.f};
#pragma unroll
        for (int ks = 0; ks < 2; ++ks) {
          s16x8 af = *(const s16x8*)(smem + (16 * kt + r16) * 144 + ks * 64 + h * 16);
          s16x8 bf = vt >= 0 ? *(const s16x8*)(smem + (80 + 16 * vt + r16) * 144 + ks * 64 + h * 16) : ones;
          dacc = mfma16(af, bf, dacc);
        }
        const int kf = 16 * kt + 4 * h, hk = kf / 20, dd = kf - 20 * hk;
        if (vt >= 0) {
          const int vf = 16 * vt + r16, hv = vf / 20, e = vf - 20 * hv;
          if (hk == hv) st16_sc1(ws, outp + ((hk * 21 + e) * 20 + dd) * 4, __builtin_bit_cast(u32x4, dacc));
        } else if (r16 == 0) {
          st16_sc1(ws, outp + ((hk * 21 + 20) * 20 + dd) * 4, __builtin_bit_cast(u32x4, dacc));
        }
      }
    }
    wait_v<0>();
    wait_l_bar();
    if (tid == 0) flag_set(ws, WS_F1 + (size_t)(g * 32 + rt) * 128, tag);
  }
  {
    WSrc wsr; wsrc_init(wsr, 3 * DM, 80 * g, 80 * g, 64);
    const char* Wb = (const char*)wqkv;
    constexpr unsigned wstride = 128u * 3 * DM * 2;
#pragma unroll
    for (int st = 0; st < 3; ++st) wissue(wsr, Wb + (size_t)st * wstride, smem + st * SLOT);
    f32x4 acc[5];
    gemm80<0>(smem, wsr, Wb, wstride, xa, acc, trb);
    s16x8 pqf[3];
    {
      unsigned pk[12];
#pragma unroll
      for (int t = 0; t < 5; ++t) {
        pk[2 * t] = pk_bf16(phi(bfr(acc[t][0])), phi(bfr(acc[t][1])));
        pk[2 * t + 1] = pk_bf16(phi(bfr(acc[t][2])), phi(bfr(acc[t][3])));
      }
      pk[10] = 0u; pk[11] = 0u;
#pragma unroll
      for (int j = 0; j < 3; ++j) pqf[j] = __builtin_bit_cast(s16x8, u32x4{pk[4 * j], pk[4 * j + 1], pk[4 * j + 2], pk[4 * j + 3]});
    }
    if (w == 0) flags_wait(ws, WS_F2 + (size_t)(4 * g) * 128, 4, tag);
    bsync();
    {
      unsigned* tb = (unsigned*)smem;
#pragma unroll
      for (int i = 0; i < 4; ++i) {
        const int gi = tid + 256 * i;
        if (gi < 840) {
          const int hd = gi / 210, e = gi - hd * 210;
          tb[gi] = __hip_atomic_load((const unsigned*)(ws + WS_KVR + (size_t)(4 * g + hd) * 1024) + e, __ATOMIC_RELAXED, __HIP_MEMORY_SCOPE_AGENT);
        }
      }
      if (tid < 4) tb[840 + tid] = 0u;
    }
    bsync();
    f32x4 num[5], den = {0.f, 0.f, 0.f, 0.f};
#pragma unroll
    for (int t = 0; t < 5; ++t) num[t] = f32x4{0.f, 0.f, 0.f, 0.f};
    auto afrag = [&](int hrow, int erow, int j) AINL -> s16x8 {
      s16x4 part[2];
#pragma unroll
      for (int hf = 0; hf < 2; ++hf) {
        int n0 = 32 * j + 16 * hf + 4 * h, hn = n0 / 20;
        int ad = (n0 < 80 && hn == hrow) ? ((hrow * 21 + erow) * 20 + (n0 - 20 * hn)) * 2 : 3360;
        part[hf] = *(const s16x4*)(smem + ad);
      }
      return cat8(part[0], part[1]);
    };
    const int jlo[5] = {0, 0, 0, 1, 1}, jhi[5] = {0, 1, 1, 2, 2};
#pragma unroll
    for (int mt = 0; mt < 5; ++mt) {
      const int e = 16 * mt + r16, he = e / 20, ee = e - 20 * he;
#pragma unroll
      for (int j = 0; j < 3; ++j)
        if (j >= jlo[mt] && j <= jhi[mt]) num[mt] = mfma16(afrag(he, ee, j), pqf[j], num[mt]);
    }
#pragma unroll
    for (int j = 0; j < 3; ++j) den = mfma16(afrag(r16 & 3, 20, j), pqf[j], den);
    float dn[4];
#pragma unroll
    for (int j = 0; j < 4; ++j) dn[j] = bfr(bfr(den[j]) + 1e-6f);
#pragma unroll
    for (int mt = 0; mt < 5; ++mt) {
      const int e0 = 16 * mt + 4 * h, hd = e0 / 20;
      const float dd = hd == 0 ? dn[0] : (hd == 1 ? dn[1] : (hd == 2 ? dn[2] : dn[3]));
      const float rc = 1.0f / dd;
      float o0 = bfr(num[mt][0]) * rc, o1 = bfr(num[mt][1]) * rc, o2 = bfr(num[mt][2]) * rc, o3 = bfr(num[mt][3]) * rc;
      *(u32x2*)(smem + 4096 + (16 * w + r16) * 176 + e0 * 2) = u32x2{pk_bf16(o0, o1), pk_bf16(o2, o3)};
    }
    bsync();
#pragma unroll
    for (int i = 0; i < 3; ++i) {
      const int id = tid + 256 * i;
      if (id < 640) {
        const int c = id >> 6, row = id & 63, f8 = 10 * g + c;
        st16_sc1(ws, WS_O + ((size_t)((rt * 4 + (row >> 4)) * 20 + (f8 >> 2)) * 1024 + ((f8 & 3) * 16 + (row & 15)) * 16),
                 *(const u32x4*)(smem + 4096 + row * 176 + c * 16));
      }
    }
    wait_v<0>();
    wait_l_bar();
    if (tid == 0) flag_set(ws, WS_F3 + (size_t)(rt * 8 + g) * 128, tag);
  }
  {
    WSrc wsr; wsrc_init(wsr, DM, 80 * g, 80 * g, 64);
    const char* Wb = (const char*)wo;
    constexpr unsigned wstride = 128u * DM * 2;
#pragma unroll
    for (int st = 0; st < 3; ++st) wissue(wsr, Wb + (size_t)st * wstride, smem + st * SLOT);
    if (w == 0) flags_wait(ws, WS_F3 + (size_t)(rt * 8) * 128, 8, tag);
    __builtin_amdgcn_s_barrier();
    const char* op = ws + WS_O + (size_t)(rt * 4 + w) * 20 * 1024 + lane * 16;
#pragma unroll
    for (int ks = 0; ks < 20; ++ks)
      asm volatile("global_load_dwordx4 %0, %1, off offset:%2 sc1" : "=v"(xa[ks]) : "v"(op + (ks >> 2) * 4096), "i"((ks & 3) * 1024) : "memory");
    f32x4 acc[5];
    gemm80<20>(smem, wsr, Wb, wstride, xa, acc, trb);
    bsync();
#pragma unroll
    for (int t = 0; t < 5; ++t)
      *(u32x2*)(smem + (16 * w + r16) * 176 + (16 * t + 4 * h) * 2) = u32x2{pk_bf16(acc[t][0], acc[t][1]), pk_bf16(acc[t][2], acc[t][3])};
    bsync();
#pragma unroll
    for (int i = 0; i < 3; ++i) {
      const int id = tid + 256 * i;
      if (id < 640) {
        const int r = id / 10, c = id - r * 10;
        *(u32x4*)(out + (size_t)(rt * 64 + r) * (2 * DM) + 80 * g + c * 8) = *(const u32x4*)(smem + r * 176 + c * 16);
      }
    }
    if (b == 0 && tid == 0) st4_sc1(ws + WS_EPOCH, tag);
  }
}

DI void role_R(int hd, char* __restrict__ ws) {
  const int tid = threadIdx.x, w = tid >> 6;
  const unsigned tag = __hip_atomic_load((const unsigned*)(ws + WS_EPOCH), __ATOMIC_RELAXED, __HIP_MEMORY_SCOPE_AGENT) + 1u;
  const int g = hd >> 2;
  if (w == 0) flags_wait(ws, WS_F1 + (size_t)(g * 32) * 128, 32, tag);
  bsync();
  if (tid < 210) {
    const u64* p = (const u64*)(ws + WS_KVP + ((size_t)g * 32 * 1680 + (hd & 3) * 420) * 4) + tid;
    u64 v[32];
#pragma unroll
    for (int i = 0; i < 32; ++i) v[i] = __hip_atomic_load(p + (size_t)i * 840, __ATOMIC_RELAXED, __HIP_MEMORY_SCOPE_AGENT);
    float s0 = 0.f, s1 = 0.f;
#pragma unroll
    for (int i = 0; i < 32; ++i) { s0 += __uint_as_float((unsigned)v[i]); s1 += __uint_as_float((unsigned)(v[i] >> 32)); }
    st4_sc1(ws + WS_KVR + (size_t)hd * 1024 + tid * 4, pk_bf16(s0, s1));
  }
  wait_v<0>();
  wait_l_bar();
  if (tid == 0) flag_set(ws, WS_F2 + (size_t)hd * 128, tag);
}

struct cplx { float r, i; };
DI cplx cmul(cplx a, cplx b) { return cplx{a.r * b.r - a.i * b.i, a.r * b.i + a.i * b.r}; }
DI cplx cexp_at(float ar, float ai, float p) {
  float m = __expf(ar * p);
  float x = ai * p;
  float k = rintf(x * 0.15915494309189535f);
  float r = fmaf(-k, 6.2831854820251465f, x);
  r = fmaf(-k, -1.7484556000744487e-07f, r);
  float rv = r * 0.15915494309189535f;
  return cplx{m * __builtin_amdgcn_cosf(rv), m * __builtin_amdgcn_sinf(rv)};
}
constexpr int KG_PITCH = 288;
constexpr int KG_WAVE = 57 * KG_PITCH;
constexpr int CV_KP = 2240;
constexpr int CV_U = 4 * CV_KP;
constexpr int CV_WAVE = CV_U + 65 * 80 + 48;
constexpr int B_T = 4 * KG_WAVE;
constexpr int B_RAW = 4 * CV_WAVE;
constexpr int B_T2 = B_RAW + 16384;

DI void role_B(char* smem, int bb, const unsigned short* __restrict__ u, const unsigned short* __restrict__ lar_,
               const unsigned short* __restrict__ aim_, const unsigned short* __restrict__ cr_,
               const unsigned short* __restrict__ ci_, const unsigned short* __restrict__ ldt_,
               const unsigned short* __restrict__ dsk_, unsigned short* __restrict__ out) {
  const int tid = threadIdx.x, w = tid >> 6, lane = tid & 63, r16 = lane & 15, h4 = lane >> 4;
  const int d0 = ((bb & 7) * 20 + (bb >> 3)) * 4, d = d0 + w;
  u32x2 ur[8];
#pragma unroll
  for (int i = 0; i < 8; ++i) ur[i] = *(const u32x2*)(u + (size_t)(tid + 256 * i) * DM + d0);
  float inv_scale;
  {
    char* sm = smem + w * KG_WAVE;
    float lar[3], aim[3], crv[3], civ[3];
#pragma unroll
    for (int c = 0; c < 3; ++c) {
      const int n = d * NMODE + 64 * c + lane;
      lar[c] = bf2f(lar_[n]); aim[c] = bf2f(aim_[n]); crv[c] = bf2f(cr_[n]); civ[c] = bf2f(ci_[n]);
    }
    const float dt = bfr(expf(bf2f(ldt_[d])));
    float s = 0.f;
#pragma unroll
    for (int c = 0; c < 3; ++c) { crv[c] *= dt; civ[c] *= dt; s += fabsf(crv[c]) + fabsf(civ[c]); }
#pragma unroll
    for (int o = 32; o >= 1; o >>= 1) s += __shfl_xor(s, o);
    const float S = 2.f * s;
    int es = (int)((__float_as_uint(S) >> 23) & 0xff);
    int se = 268 - es; se = se < 1 ? 1 : (se > 253 ? 253 : se);
    const float sc = __uint_as_float((unsigned)se << 23);
    inv_scale = __uint_as_float((unsigned)(254 - se) << 23);
    f32x4 acc[3];
#pragma unroll
    for (int t = 0; t < 3; ++t) acc[t] = f32x4{0.f, 0.f, 0.f, 0.f};
    char* wp = sm + 4 * lane;
    const char* ap = sm + r16 * KG_PITCH + h4 * 16;
#pragma unroll 1
    for (int c = 0; c < DBG_NCH; ++c) {
      const float larc = c == 0 ? lar[0] : (c == 1 ? lar[1] : lar[2]);
      const float aimc = c == 0 ? aim[0] : (c == 1 ? aim[1] : aim[2]);
      const float crc = c == 0 ? crv[0] : (c == 1 ? crv[1] : crv[2]);
      const float cic = c == 0 ? civ[0] : (c == 1 ? civ[1] : civ[2]);
      const float a = bfr(expf(larc));
      const float ar = -a * dt, ai = aimc * dt;
      const cplx cc = cplx{crc * sc, cic * sc};
      auto putA = [&](int row, cplx v) AINL { *(unsigned*)(wp + row * KG_PITCH) = pk_f16(v.r, -v.i); };
      auto putB = [&](int row, cplx v) AINL { *(unsigned*)(wp + (41 + row) * KG_PITCH) = pk_f16(v.r, v.i); };
      {
        cplx f[16];
        f[0] = cplx{1.f, 0.f}; f[1] = cexp_at(ar, ai, 8.f);
        f[2] = cmul(f[1], f[1]); f[3] = cmul(f[2], f[1]); f[4] = cmul(f[2], f[2]);
        f[5] = cmul(f[4], f[1]); f[6] = cmul(f[4], f[2]); f[7] = cmul(f[4], f[3]); f[8] = cmul(f[4], f[4]);
#pragma unroll
        for (int j = 1; j < 8; ++j) f[8 + j] = cmul(f[8], f[j]);
#pragma unroll
        for (int j = 0; j < 16; ++j) putB(j, f[j]);
      }
      cplx e[8], g[8], cg[17];
      e[0] = cplx{1.f, 0.f}; e[1] = cexp_at(ar, ai, 1.f);
      e[2] = cmul(e[1], e[1]); e[3] = cmul(e[2], e[1]); e[4] = cmul(e[2], e[2]);
      e[5] = cmul(e[4], e[1]); e[6] = cmul(e[4], e[2]); e[7] = cmul(e[4], e[3]);
      g[0] = cplx{1.f, 0.f}; g[1] = cexp_at(ar, ai, 128.f);
      g[2] = cmul(g[1], g[1]); g[3] = cmul(g[2], g[1]); g[4] = cmul(g[2], g[2]);
      g[5] = cmul(g[4], g[1]); g[6] = cmul(g[4], g[2]); g[7] = cmul(g[4], g[3]);
      const cplx g8 = cexp_at(ar, ai, 1024.f);
      cg[0] = cc; cg[8] = cmul(cc, g8); cg[16] = cmul(cg[8], g8);
#pragma unroll
      for (int j = 1; j < 8; ++j) { cg[j] = cmul(cc, g[j]); cg[8 + j] = cmul(cg[8], g[j]); }
#pragma unroll
      for (int i = 0; i < 16; ++i) putA(i, (i & 7) ? cmul(cg[i >> 3], e[i & 7]) : cg[i >> 3]);
#pragma unroll
      for (int i = 0; i < 8; ++i) putA(16 + i, (i & 3) ? cmul(cg[2 + (i >> 2)], e[2 * (i & 3)]) : cg[2 + (i >> 2)]);
#pragma unroll
      for (int i = 0; i < 8; ++i) putA(24 + i, (i & 1) ? cmul(cg[4 + (i >> 1)], e[4]) : cg[4 + (i >> 1)]);
#pragma unroll
      for (int i = 0; i < 8; ++i) putA(32 + i, cg[8 + i]);
      putA(40, cg[16]);
      s16x8 bf[4], af[4][3];
#pragma unroll
      for (int ks = 0; ks < 4; ++ks) {
        bf[ks] = *(const s16x8*)(ap + 41 * KG_PITCH + ks * 64);
#pragma unroll
        for (int t = 0; t < 3; ++t) af[ks][t] = *(const s16x8*)(ap + 16 * t * KG_PITCH + ks * 64);
      }
#pragma unroll
      for (int ks = 0; ks < 4; ++ks)
#pragma unroll
        for (int t = 0; t < 3; ++t) acc[t] = mfma16h(af[ks][t], bf[ks], acc[t]);
    }
    unsigned short* T = (unsigned short*)(smem + B_T2 + w * 1408);
#pragma unroll
    for (int t = 0; t < 3; ++t)
#pragma unroll
      for (int rg = 0; rg < 4; ++rg) {
        const int i = 4 * h4 + rg, j = r16;
        int idx;
        if (t == 0) idx = (i & 7) + 128 * (i >> 3) + 8 * j;
        else if (t == 1) idx = i < 8 ? 256 + (i & 3) + 64 * ((i >> 2) & 1) + 4 * j : 384 + (i & 1) + 32 * ((i >> 1) & 3) + 2 * j;
        else idx = i < 8 ? 512 + 16 * i + j : ((i == 8 && j == 0) ? 640 : 700);
        T[idx] = (unsigned short)(pk_f16(2.f * acc[t][rg], 0.f) & 0xffff);
      }
  }
  bsync();
  {
    unsigned short* rt_ = (unsigned short*)(smem + B_RAW);
#pragma unroll
    for (int i = 0; i < 8; ++i) {
      const int l = tid + 256 * i;
      rt_[l] = (unsigned short)(ur[i][0] & 0xffff); rt_[2048 + l] = (unsigned short)(ur[i][0] >> 16);
      rt_[4096 + l] = (unsigned short)(ur[i][1] & 0xffff); rt_[6144 + l] = (unsigned short)(ur[i][1] >> 16);
    }
  }
  bsync();
  char* cb = smem + w * CV_WAVE;
  const unsigned coff = w * CV_WAVE;
  float uinv;
  {
    const char* raw = smem + B_RAW + w * 4096;
    u32x4 uv[4];
    unsigned mx = 0;
#pragma unroll
    for (int c = 0; c < 4; ++c) {
      uv[c] = *(const u32x4*)(raw + (lane + 64 * c) * 16);
#pragma unroll
      for (int j = 0; j < 4; ++j) mx = max(mx, max(uv[c][j] & 0x7fffu, (uv[c][j] >> 16) & 0x7fffu));
    }
#pragma unroll
    for (int o = 32; o >= 1; o >>= 1) mx = max(mx, (unsigned)__shfl_xor((int)mx, o));
    int eu = (int)(mx >> 7);
    int se = 268 - eu; se = se > 253 ? 253 : (se < 1 ? 1 : se);
    const float usc = __uint_as_float((unsigned)se << 23);
    uinv = __uint_as_float((unsigned)(254 - se) << 23);
#pragma unroll
    for (int c = 0; c < 4; ++c) {
      const int cidx = lane + 64 * c;
      u32x4 o4;
#pragma unroll
      for (int j = 0; j < 4; ++j) o4[j] = pk_f16(lo16(uv[c][j]) * usc, hi16(uv[c][j]) * usc);
      *(u32x4*)(cb + CV_U + ((cidx >> 2) + 1) * 80 + (cidx & 3) * 16) = o4;
    }
    if (lane < 5) *(u32x4*)(cb + CV_U + lane * 16) = u32x4{0u, 0u, 0u, 0u};
  }
  const unsigned short* T = (const unsigned short*)(smem + B_T2 + w * 1408);
  const int i32 = lane & 31, h = lane >> 5, blk = (lane >> 4) & 1, q = (lane & 15) >> 2, p = lane & 3;
  const unsigned ab = coff + q * CV_KP + 2 * (32 + 16 * blk - 8 * h + 4 * p) - 40;
  const unsigned ubase = coff + CV_U + 16 * h;
  f32x16 acc0 = {}, acc1 = {};
#pragma unroll
  for (int pass = 0; pass < DBG_NPASS; ++pass) {
    const int tb = pass * 1024;
#pragma unroll
    for (int it = 0; it < 3; ++it) {
      const int qc = lane + 64 * it;
      if (qc < 132) {
        const int t0 = tb - 32 + 8 * qc;
        unsigned v[12];
#pragma unroll
        for (int j = 0; j < 12; ++j) {
          const int t = t0 - 4 + j;
          int idx;
          if (t < 256) idx = t;
          else if (t < 512) idx = 128 + ((t + ((t >> 1) & 1)) >> 1);
          else if (t < 1024) idx = 256 + ((t + 1 + ((t >> 2) & 1)) >> 2);
          else idx = 384 + ((t + 3 + ((t >> 3) & 1)) >> 3);
          v[j] = t >= 0 ? (unsigned)T[idx < 0 ? 0 : idx] : 0u;
        }
        unsigned ev[6], od[5];
#pragma unroll
        for (int i = 0; i < 6; ++i) ev[i] = v[2 * i] | (v[2 * i + 1] << 16);
#pragma unroll
        for (int i = 0; i < 5; ++i) od[i] = v[2 * i + 1] | (v[2 * i + 2] << 16);
        char* dst = cb + 16 * qc;
        *(u32x4*)(dst) = u32x4{ev[2], ev[3], ev[4], ev[5]};
        *(u32x4*)(dst + CV_KP) = u32x4{od[1], od[2], od[3], od[4]};
        *(u32x4*)(dst + 2 * CV_KP) = u32x4{ev[1], ev[2], ev[3], ev[4]};
        *(u32x4*)(dst + 3 * CV_KP) = u32x4{od[0], od[1], od[2], od[3]};
      }
    }
    s16x8 fa[2][6];
    auto ldfr = [&](int m, s16x8 (&f)[6]) AINL {
      const unsigned a0 = ab + 64 * m;
      const int r0 = i32 - m + 1;
      const unsigned u0 = ubase + 80 * (r0 > 0 ? r0 : 0);
      const unsigned u1 = ubase + 80 * (i32 - m + 33);
#pragma unroll
      for (int ks = 0; ks < 2; ++ks) {
        const unsigned ao = a0 + 32 * (1 - ks);
        f[ks] = cat8(tr_read(smem, ao + 8), tr_read(smem, ao));
        f[2 + ks] = *(const s16x8*)(smem + u0 + 32 * ks);
        if (pass == 0) f[4 + ks] = *(const s16x8*)(smem + u1 + 32 * ks);
      }
    };
    auto mm = [&](s16x8 (&f)[6]) AINL {
#pragma unroll
      for (int ks = 0; ks < 2; ++ks) {
        if (pass == 0) { acc0 = mfma32h(f[ks], f[2 + ks], acc0); acc1 = mfma32h(f[ks], f[4 + ks], acc1); }
        else acc1 = mfma32h(f[ks], f[2 + ks], acc1);
      }
    };
    ldfr(0, fa[0]);
#pragma unroll 1
    for (int m = 0; m < 32; m += 2) {
      ldfr(m + 1, fa[1]);
      __builtin_amdgcn_sched_barrier(0);
      mm(fa[0]);
      __builtin_amdgcn_sched_barrier(0);
      ldfr(m + 2 < 32 ? m + 2 : 0, fa[0]);
      __builtin_amdgcn_sched_barrier(0);
      mm(fa[1]);
      __builtin_amdgcn_sched_barrier(0);
    }
  }
  {
    const float inv = inv_scale * uinv;
    const float dk = bf2f(dsk_[d]);
    char* raw = smem + B_RAW + w * 4096;
#pragma unroll
    for (int c = 0; c < 2; ++c)
#pragma unroll
      for (int g4 = 0; g4 < 4; ++g4) {
        const int l = 1024 * c + 32 * i32 + 8 * g4 + 4 * h;
        const u32x2 uu = *(const u32x2*)(raw + l * 2);
        float r[4];
#pragma unroll
        for (int j = 0; j < 4; ++j) {
          const float y = bfr((c ? acc1[4 * g4 + j] : acc0[4 * g4 + j]) * inv);
          const float uf = (j & 1) ? hi16(uu[j >> 1]) : lo16(uu[j >> 1]);
          r[j] = y + bfr(uf * dk);
        }
        *(u32x2*)(raw + l * 2) = u32x2{pk_bf16(r[0], r[1]), pk_bf16(r[2], r[3])};
      }
  }
  bsync();
  {
    const unsigned short* rt_ = (const unsigned short*)(smem + B_RAW);
#pragma unroll
    for (int i = 0; i < 8; ++i) {
      const int l = tid + 256 * i;
      const u32x2 v = {(unsigned)rt_[l] | ((unsigned)rt_[2048 + l] << 16), (unsigned)rt_[4096 + l] | ((unsigned)rt_[6144 + l] << 16)};
      *(u32x2*)(out + (size_t)l * (2 * DM) + DM + d0) = v;
    }
  }
}

__global__ NOPK __launch_bounds__(256, 2) void k_fused(
    const unsigned short* __restrict__ x, const unsigned short* __restrict__ wqkv, const unsigned short* __restrict__ wo,
    const unsigned short* __restrict__ u, const unsigned short* __restrict__ lar, const unsigned short* __restrict__ aim,
    const unsigned short* __restrict__ cr, const unsigned short* __restrict__ ci, const unsigned short* __restrict__ ldt,
    const unsigned short* __restrict__ dsk, char* __restrict__ ws, unsigned short* __restrict__ out) {
  extern __shared__ __attribute__((aligned(16))) char smem[];
  const int b = blockIdx.x;
  if (b < NB_A) role_A(smem, b, x, wqkv, wo, ws, out);
  else if (b < NB_A + NB_R) role_R(b - NB_A, ws);
  else role_B(smem, b - NB_A - NB_R, u, lar, aim, cr, ci, ldt, dsk, out);
}

extern "C" void kernel_launch(void* const* d_in, const int* in_sizes, int n_in, void* d_out, int out_size, void* d_ws,
                              size_t ws_size, hipStream_t stream) {
  constexpr int LDS = 81920;
  (void)hipFuncSetAttribute((const void*)k_fused, hipFuncAttributeMaxDynamicSharedMemorySize, LDS);
  k_fused<<<NB_A + NB_R + NB_B, 256, LDS, stream>>>(
      (const unsigned short*)d_in[0], (const unsigned short*)d_in[1], (const unsigned short*)d_in[2],
      (const unsigned short*)d_in[3], (const unsigned short*)d_in[4], (const unsigned short*)d_in[5],
      (const unsigned short*)d_in[6], (const unsigned short*)d_in[7], (const unsigned short*)d_in[8],
      (const unsigned short*)d_in[9], (char*)d_ws, (unsigned short*)d_out);
}
```

```cpp
#include <hip/hip_runtime.h>
#include <hip/hip_bf16.h>
#include <stdint.h>

#define DI __device__ __forceinline__
#define NOPK __attribute__((target("no-packed-fp32-ops")))
#define AINL __attribute__((always_inline))
#define LDS_AS __attribute__((address_space(3)))
#define GLB_AS __attribute__((address_space(1)))

typedef __attribute__((ext_vector_type(8))) short s16x8;
typedef __attribute__((ext_vector_type(4))) short s16x4;
typedef __attribute__((ext_vector_type(8))) _Float16 f16x8;
typedef __attribute__((ext_vector_type(2))) _Float16 f16x2;
typedef __attribute__((ext_vector_type(8))) __bf16 b16x8;
typedef __attribute__((ext_vector_type(2))) __bf16 b16x2;
typedef __attribute__((ext_vector_type(4))) float f32x4;
typedef __attribute__((ext_vector_type(2))) float f32x2;
typedef __attribute__((ext_vector_type(16))) float f32x16;
typedef __attribute__((ext_vector_type(4))) unsigned u32x4;
typedef __attribute__((ext_vector_type(2))) unsigned u32x2;
typedef unsigned long long u64;

#define DBG_NPASS 2
#define DBG_NCH 3
constexpr int SEQ = 2048, DM = 640, NMODE = 192;
constexpr int NB_A = 256, NB_R = 32, NB_B = 160;

constexpr size_t WS_EPOCH = 0;
constexpr size_t WS_F2  = 4096;
constexpr size_t WS_F3  = WS_F2 + 32 * 128;
constexpr size_t WS_F4  = WS_F3 + 256 * 128;
constexpr size_t WS_KVP = WS_F4 + 320 * 128;
constexpr size_t WS_Z   = WS_KVP + (size_t)8 * 32 * 840 * 16;
constexpr size_t WS_PQ  = WS_Z + 4096;
constexpr size_t WS_MT  = WS_PQ + (size_t)SEQ * DM * 2;
constexpr size_t WS_UT  = WS_MT + (size_t)DM * DM * 2;

#define STAMP(i) do { } while (0)
#define STAMP_DECL
#define STAMP_FLUSH() do { } while (0)
template <int V> struct IC { static constexpr int value = V; };
template <int N, int I = 0, class F> DI void static_for(F& f) {
  if constexpr (I < N) { f(IC<I>{}); static_for<N, I + 1>(f); }
}
DI float bf2f(unsigned short h) { return __uint_as_float((unsigned)h << 16); }
DI unsigned pk_bf16(float a, float b) { f32x2 v = {a, b}; return __builtin_bit_cast(unsigned, __builtin_convertvector(v, b16x2)); }
DI unsigned pk_f16(float a, float b) { f32x2 v = {a, b}; return __builtin_bit_cast(unsigned, __builtin_convertvector(v, f16x2)); }
DI float bfr(float x) { return __uint_as_float(pk_bf16(x, 0.f) << 16); }
DI float lo16(unsigned p) { return __uint_as_float(p << 16); }
DI float hi16(unsigned p) { return __uint_as_float(p & 0xffff0000u); }
DI unsigned opq(unsigned x) { asm("" : "+v"(x)); return x; }

DI f32x4 mfma16(s16x8 a, s16x8 b, f32x4 c) {
  return __builtin_amdgcn_mfma_f32_16x16x32_bf16(__builtin_bit_cast(b16x8, a), __builtin_bit_cast(b16x8, b), c, 0, 0, 0);
}
DI f32x4 mfma16h(s16x8 a, s16x8 b, f32x4 c) {
  return __builtin_amdgcn_mfma_f32_16x16x32_f16(__builtin_bit_cast(f16x8, a), __builtin_bit_cast(f16x8, b), c, 0, 0, 0);
}
DI f32x16 mfma32h(s16x8 a, s16x8 b, f32x16 c) {
  return __builtin_amdgcn_mfma_f32_32x32x16_f16(__builtin_bit_cast(f16x8, a), __builtin_bit_cast(f16x8, b), c, 0, 0, 0);
}
DI s16x4 tr_read(const char* smem, unsigned off) {
  return __builtin_amdgcn_ds_read_tr16_b64_v4i16((LDS_AS s16x4*)(smem + off));
}
DI s16x8 cat8(s16x4 a, s16x4 b) { return __builtin_shufflevector(a, b, 0, 1, 2, 3, 4, 5, 6, 7); }

DI void bsync() {
  __builtin_amdgcn_fence(__ATOMIC_RELEASE, "workgroup");
  __builtin_amdgcn_s_barrier();
  __builtin_amdgcn_fence(__ATOMIC_ACQUIRE, "workgroup");
}
template <int N> DI void wait_v() { asm volatile("s_waitcnt vmcnt(%0)" ::"n"(N) : "memory"); }
DI void wait_l_bar() { asm volatile("s_waitcnt lgkmcnt(0)" ::: "memory"); __builtin_amdgcn_s_barrier(); }
DI void glds16(const void* g, char* l) {
  __builtin_amdgcn_global_load_lds((const GLB_AS unsigned*)g, (LDS_AS unsigned*)l, 16, 0, 0);
}
DI void glds16_sc1(const void* g, char* l) { glds16(g, l); }
DI void st16_sc1(char* ws, size_t off, u32x4 v) {
  __builtin_amdgcn_raw_buffer_store_b128(v, __builtin_amdgcn_make_buffer_rsrc(ws, 0, 64u << 20, 0x00020000), (int)off, 0, 16);
}
DI void st4_sc1(void* p, unsigned v) { __hip_atomic_store((unsigned*)p, v, __ATOMIC_RELAXED, __HIP_MEMORY_SCOPE_AGENT); }
DI u64 flag_val(unsigned tag) { return ((u64)tag << 32) | (u64)(tag ^ 0x9e3779b9u); }
DI void flag_set(char* ws, size_t off, unsigned tag) {
  __hip_atomic_store((u64*)(ws + off), flag_val(tag), __ATOMIC_RELAXED, __HIP_MEMORY_SCOPE_AGENT);
}
DI void flags_wait(const char* ws, size_t off, int n, unsigned tag) {
  const int lane = threadIdx.x & 63;
  const u64 want = flag_val(tag);
  const u64* p = (const u64*)(ws + off + (size_t)(lane < n ? lane : 0) * 128);
  for (unsigned spins = 0; spins < (1u << 20); ++spins) {
    const u64 v = __hip_atomic_load(p, __ATOMIC_RELAXED, __HIP_MEMORY_SCOPE_AGENT);
    if (__all(v == want)) break;
    __builtin_amdgcn_s_sleep(2);
  }
}

DI float phi(float t) {
  float a = bfr(t + 1.0f);
  float e = bfr(__expf(t));
  float s = t > 0.f ? a : e;
  return bfr(s + 1e-6f);
}

constexpr int SLOT = 20480;
constexpr int XS = 3 * SLOT;

struct WSrc { unsigned off[5]; };
DI void wsrc_init(WSrc& s, int ldw, int colA, int colB, int kB) {
  const int w = threadIdx.x >> 6, lane = threadIdx.x & 63;
#pragma unroll
  for (int i = 0; i < 5; ++i) {
    int c = (w + 4 * i) * 64 + lane;
    int unit = c >= 640, cu = c - unit * 640;
    int pos = cu / 10, ch = cu - pos * 10;
    int k = pos ^ (((pos >> 3) & 1) << 2);
    s.off[i] = (unsigned)((k + (unit ? kB : 0)) * ldw + (unit ? colB : colA) + ch * 8) * 2u;
  }
}
DI void wissue(const WSrc& s, const char* Wst, char* slot) {
  char* dst = slot + (threadIdx.x >> 6) * 1024;
#pragma unroll
  for (int i = 0; i < 5; ++i) glds16(Wst + s.off[i], dst + i * 4096);
}

DI void tissue(const char* Wt, int st, char* slot) {
  const int w = threadIdx.x >> 6, lane = threadIdx.x & 63;
#pragma unroll
  for (int i = 0; i < 5; ++i) glds16_sc1(Wt + st * SLOT + (w + 4 * i) * 1024 + lane * 16, slot + (w + 4 * i) * 1024);
}
template <int V0, int V1, int V2, int V3, int V4, class HOOK>
DI void gemm80(char* smem, const char* Wt, s16x8 (&xa)[20], f32x4 (&acc)[5], const unsigned (&trb)[2], HOOK hook) {
  const int w = threadIdx.x >> 6, lane = threadIdx.x & 63;
#pragma unroll
  for (int t = 0; t < 5; ++t) acc[t] = f32x4{0.f, 0.f, 0.f, 0.f};
#pragma unroll
  for (int st = 0; st < 5; ++st) {
    if (st == 0) wait_v<V0>(); else if (st == 1) wait_v<V1>(); else if (st == 2) wait_v<V2>();
    else if (st == 3) wait_v<V3>(); else wait_v<V4>();
    wait_l_bar();
    hook(st);
    const unsigned sb = (st & 3) * SLOT;
    s16x8 wf[2][5];
#pragma unroll
    for (int t = 0; t < 5; ++t) wf[0][t] = cat8(tr_read(smem, sb + t * 32 + trb[0]), tr_read(smem, sb + t * 32 + trb[1]));
#pragma unroll
    for (int kk = 0; kk < 4; ++kk) {
      if (kk < 3) {
        const unsigned o = sb + ((kk + 1) >> 1) * 10240 + ((kk + 1) & 1) * 5120;
#pragma unroll
        for (int t = 0; t < 5; ++t) wf[(kk + 1) & 1][t] = cat8(tr_read(smem, o + t * 32 + trb[0]), tr_read(smem, o + t * 32 + trb[1]));
      }
#pragma unroll
      for (int t = 0; t < 5; ++t) acc[t] = mfma16(wf[kk & 1][t], xa[st * 4 + kk], acc[t]);
      if (st == 1) {
        glds16_sc1(Wt + 4 * SLOT + (w + 4 * kk) * 1024 + lane * 16, smem + (w + 4 * kk) * 1024);
        if (kk == 3) glds16_sc1(Wt + 4 * SLOT + (w + 16) * 1024 + lane * 16, smem + (w + 16) * 1024);
      }
      __builtin_amdgcn_sched_barrier(0);
    }
  }
}

constexpr int NIT = 19;
DI constexpr int item_typ(int it) { return it >= 14 ? 1 : ((it == 0 || it == 3 || it == 7 || it == 10) ? 0 : 2); }
DI constexpr int item_idx(int it) {
  return it >= 14 ? it - 14 : (it == 0 ? 0 : it == 3 ? 1 : it == 7 ? 2 : it == 10 ? 3 : it < 3 ? it - 1 : it < 7 ? it - 2 : it < 10 ? it - 3 : it - 4);
}
DI void role_A(char* smem, int b, const unsigned short* __restrict__ x, const unsigned short* __restrict__ wqkv,
               const unsigned short* __restrict__ wo, char* __restrict__ ws, unsigned short* __restrict__ out) {
  const int tid = threadIdx.x, w = tid >> 6, lane = tid & 63, r16 = lane & 15, h = lane >> 4;
  const int xcd = b & 7, idx = b >> 3;
  const int g = (xcd & 1) * 4 + (idx & 3), rt = (xcd >> 1) * 8 + (idx >> 2);
  STAMP_DECL
  STAMP(0);
  const unsigned tag = __hip_atomic_load((const unsigned*)(ws + WS_EPOCH), __ATOMIC_RELAXED, __HIP_MEMORY_SCOPE_AGENT) + 1u;
  unsigned trb[2];
  {
    const int q = r16 >> 2, p = lane & 3;
#pragma unroll
    for (int t = 0; t < 2; ++t) { int k = 8 * h + 4 * t + q; trb[t] = (k ^ (((k >> 3) & 1) << 2)) * 160 + p * 8; }
  }
  s16x8 xa[20];
  const u64 want = flag_val(tag);
  {
    WSrc wq_, wk_;
    wsrc_init(wq_, 3 * DM, 80 * g, 80 * g, 64);
    wsrc_init(wk_, 3 * DM, DM + 80 * g, 2 * DM + 80 * g, 0);
    const char* Wb = (const char*)wqkv;
    unsigned xoff[5];
#pragma unroll
    for (int i = 0; i < 5; ++i) {
      const int c = (w + 4 * i) * 64 + lane, row = c / 20, ch = c - row * 20;
      xoff[i] = (unsigned)(rt * 64 + row) * (DM * 2) + ch * 16;
    }
    auto piece = [&](int it, int i) AINL {
      char* dst = smem + (it & 3) * SLOT + w * 1024 + i * 4096;
      if (item_typ(it) == 0) glds16((const char*)x + xoff[i] + item_idx(it) * 320, dst);
      else if (item_typ(it) == 1) glds16(Wb + (size_t)item_idx(it) * (128u * 3 * DM * 2) + wq_.off[i], dst);
      else glds16(Wb + (size_t)item_idx(it) * (64u * 3 * DM * 2) + wk_.off[i], dst);
    };
#pragma unroll
    for (int it = 0; it < 3; ++it)
#pragma unroll
      for (int i = 0; i < 5; ++i) piece(it, i);
    f32x4 acc[10];
#pragma unroll
    for (int t = 0; t < 10; ++t) acc[t] = f32x4{0.f, 0.f, 0.f, 0.f};
    const unsigned xrd = (16 * w + r16) * 320 + h * 16;
    const auto rsrc = __builtin_amdgcn_make_buffer_rsrc(ws, 0, 64u << 20, 0x00020000);
    auto step = [&](auto itc) AINL {
      constexpr int it = decltype(itc)::value;
      if (it <= 13) wait_v<10>(); else if (it <= 16) wait_v<20>(); else if (it == 17) wait_v<5>(); else wait_v<0>();
      wait_l_bar();
      if (it == 0) STAMP(1);
      const unsigned sb = (it & 3) * SLOT;
      if constexpr (item_typ(it) == 0) {
#pragma unroll
        for (int kk = 0; kk < 5; ++kk) xa[5 * item_idx(it) + kk] = *(const s16x8*)(smem + sb + xrd + kk * 64);
#pragma unroll
        for (int i = 0; i < 5; ++i) piece(it + 3, i);
      } else if constexpr (item_typ(it) == 1) {
        constexpr int st = item_idx(it);
        s16x8 wf[2][5];
#pragma unroll
        for (int t = 0; t < 5; ++t) wf[0][t] = cat8(tr_read(smem, sb + t * 32 + trb[0]), tr_read(smem, sb + t * 32 + trb[1]));
#pragma unroll
        for (int kk = 0; kk < 4; ++kk) {
          if (kk < 3) {
            const unsigned o = sb + ((kk + 1) >> 1) * 10240 + ((kk + 1) & 1) * 5120;
#pragma unroll
            for (int t = 0; t < 5; ++t) wf[(kk + 1) & 1][t] = cat8(tr_read(smem, o + t * 32 + trb[0]), tr_read(smem, o + t * 32 + trb[1]));
          }
#pragma unroll
          for (int t = 0; t < 5; ++t) acc[t] = mfma16(wf[kk & 1][t], xa[st * 4 + kk], acc[t]);
          if (it + 3 < NIT) { piece(it + 3, kk); if (kk == 3) piece(it + 3, 4); }
          __builtin_amdgcn_sched_barrier(0);
        }
      } else {
        constexpr int st = item_idx(it);
        s16x8 wf[2][5];
#pragma unroll
        for (int t = 0; t < 5; ++t) wf[0][t] = cat8(tr_read(smem, sb + t * 32 + trb[0]), tr_read(smem, sb + t * 32 + trb[1]));
#pragma unroll
        for (int j = 0; j < 4; ++j) {
          if (j < 3) {
            const unsigned o = sb + ((j + 1) & 1) * 10240 + ((j + 1) >> 1) * 5120;
#pragma unroll
            for (int t = 0; t < 5; ++t) wf[(j + 1) & 1][t] = cat8(tr_read(smem, o + t * 32 + trb[0]), tr_read(smem, o + t * 32 + trb[1]));
          }
#pragma unroll
          for (int t = 0; t < 5; ++t) acc[(j & 1) * 5 + t] = mfma16(xa[2 * st + (j >> 1)], wf[j & 1][t], acc[(j & 1) * 5 + t]);
          piece(it + 3, j); if (j == 3) piece(it + 3, 4);
          __builtin_amdgcn_sched_barrier(0);
        }
      }
      if constexpr (it == 13) {
        STAMP(2);
        char* im = smem + sb;
        wait_l_bar();
#pragma unroll
        for (int t = 0; t < 10; ++t) {
          float v0 = bfr(acc[t][0]), v1 = bfr(acc[t][1]), v2 = bfr(acc[t][2]), v3 = bfr(acc[t][3]);
          if (t < 5) { v0 = phi(v0); v1 = phi(v1); v2 = phi(v2); v3 = phi(v3); }
          *(u32x2*)(im + (16 * t + r16) * 128 + (((2 * w + (h >> 1)) ^ (r16 & 7)) << 4) + (h & 1) * 8) = u32x2{pk_bf16(v0, v1), pk_bf16(v2, v3)};
          acc[t] = f32x4{0.f, 0.f, 0.f, 0.f};
        }
        wait_l_bar();
        const unsigned outp = (unsigned)(WS_KVP + (size_t)(g * 32 + rt) * 840 * 16);
        const unsigned tlo = (unsigned)want, thi = (unsigned)(want >> 32);
        s16x8 ones;
#pragma unroll
        for (int i = 0; i < 8; ++i) ones[i] = (short)0x3f80;
#pragma unroll
        for (int jj = 0; jj < 5; ++jj) {
          const int j = 4 * jj + w;
          const int kt = j < 13 ? (j + 1 + (j >= 2) + 0) / 3 + (j >= 13 ? 0 : 0) : (j < 18 ? j - 13 : 0);
          const int ktb = (j + 1) / 3, vtb = j - 2 * ktb - (ktb > 0 ? 0 : 0);
          const int kq = j < 13 ? ktb : (j < 18 ? j - 13 : 0);
          const int vq = j < 13 ? (j + 1) - 3 * ktb + ktb - 1 : -1;
          (void)kt; (void)vtb;
          f32x4 dacc = {0.f, 0.f, 0.f, 0.f};
#pragma unroll
          for (int ks = 0; ks < 2; ++ks) {
            const unsigned co = ((4 * ks + h) ^ (r16 & 7)) << 4;
            s16x8 af = *(const s16x8*)(im + (16 * kq + r16) * 128 + co);
            s16x8 bf = *(const s16x8*)(im + (80 + 16 * (vq < 0 ? 0 : vq) + r16) * 128 + co);
            dacc = mfma16(af, vq >= 0 ? bf : ones, dacc);
          }
          const int kf = 16 * kq + 4 * h, hk = kf / 20, dd = kf - 20 * hk;
          const int vf = 16 * vq + r16, hv = vq >= 0 ? vf / 20 : hk, e = vq >= 0 ? vf - 20 * hv : 20;
          const bool ok = j < 18 && hk == hv && (vq >= 0 || r16 == 0);
          const unsigned o = ok ? outp + (unsigned)((hk * 21 + e) * 10 + (dd >> 1)) * 16u : 0x7ff00000u;
          __builtin_amdgcn_raw_buffer_store_b128(u32x4{__float_as_uint(dacc[0]), __float_as_uint(dacc[1]), tlo, thi}, rsrc, (int)o, 0, 16);
          __builtin_amdgcn_raw_buffer_store_b128(u32x4{__float_as_uint(dacc[2]), __float_as_uint(dacc[3]), tlo, thi}, rsrc, (int)(o + 16u), 0, 16);
        }
        __builtin_amdgcn_sched_barrier(0);
        STAMP(3);
      }
    };
    static_for<NIT>(step);
#pragma unroll
    for (int t = 0; t < 5; ++t) {
      const int f = 80 * g + 16 * t + 4 * h, f8 = f >> 3;
      const u32x2 v = {pk_bf16(phi(bfr(acc[t][0])), phi(bfr(acc[t][1]))), pk_bf16(phi(bfr(acc[t][2])), phi(bfr(acc[t][3])))};
      __builtin_amdgcn_raw_buffer_store_b64(v, rsrc,
          (int)(WS_PQ + ((size_t)((rt * 4 + w) * 20 + (f8 >> 2)) * 1024 + ((f8 & 3) * 16 + r16) * 16 + (f & 4) * 2)), 0, 16);
    }
    wait_v<0>();
    wait_l_bar();
    if (tid == 0) flag_set(ws, WS_F3 + (size_t)(rt * 8 + g) * 128, tag);
    STAMP(4);
    flags_wait(ws, WS_F3 + (size_t)(rt * 8) * 128, 8, tag);
    {
      const char* op = ws + WS_PQ + (size_t)(rt * 4 + w) * 20 * 1024 + lane * 16;
#pragma unroll
      for (int ks = 0; ks < 20; ++ks)
        asm volatile("global_load_dwordx4 %0, %1, off offset:%2" : "=v"(xa[ks]) : "v"(op + (ks >> 2) * 4096), "i"((ks & 3) * 1024) : "memory");
    }
  }
  {
    wait_l_bar();
    {
      const u64* gp = (const u64*)(ws + WS_Z);
      u64 v0, v1;
      for (unsigned spins = 0; spins < (1u << 20); ++spins) {
        v0 = __hip_atomic_load(gp + tid, __ATOMIC_RELAXED, __HIP_MEMORY_SCOPE_AGENT);
        v1 = __hip_atomic_load(gp + 256 + (tid & 63), __ATOMIC_RELAXED, __HIP_MEMORY_SCOPE_AGENT);
        if (__all((unsigned)(v0 >> 32) == tag && (unsigned)(v1 >> 32) == tag)) break;
        __builtin_amdgcn_s_sleep(1);
      }
      ((unsigned*)smem)[tid] = (unsigned)v0;
      if (tid < 64) ((unsigned*)smem)[256 + tid] = (unsigned)v1;
    }
    wait_v<0>();
#pragma unroll
    for (int ks = 0; ks < 20; ++ks) asm volatile("" : "+v"(xa[ks]));
    bsync();
    STAMP(5);
    float* rcp = (float*)(smem + 2048 + w * 2176);
#pragma unroll
    for (int T = 0; T < 2; ++T) {
      f32x4 dacc = {0.f, 0.f, 0.f, 0.f};
      const int fbase = 20 * (16 * T + r16);
#pragma unroll
      for (int k2 = 0; k2 < 10; ++k2) {
        const int ks = 10 * T + k2, f0 = 32 * ks + 8 * h;
        u32x4 zz = *(const u32x4*)(smem + f0 * 2);
#pragma unroll
        for (int j = 0; j < 4; ++j) {
          const unsigned d0 = (unsigned)(f0 + 2 * j - fbase);
          zz[j] = d0 < 20u ? zz[j] : 0u;
        }
        dacc = mfma16(__builtin_bit_cast(s16x8, zz), xa[ks], dacc);
      }
#pragma unroll
      for (int rg = 0; rg < 4; ++rg) rcp[r16 * 34 + 16 * T + 4 * h + rg] = 1.0f / bfr(bfr(dacc[rg]) + 1e-6f);
    }
#pragma unroll
    for (int ks = 0; ks < 20; ++ks) {
      const int f0 = 32 * ks + 8 * h;
      u32x4 v = __builtin_bit_cast(u32x4, xa[ks]);
#pragma unroll
      for (int j = 0; j < 4; ++j) {
        const float rc = rcp[r16 * 34 + (f0 + 2 * j) / 20];
        v[j] = pk_bf16(lo16(v[j]) * rc, hi16(v[j]) * rc);
      }
      xa[ks] = __builtin_bit_cast(s16x8, v);
    }
    STAMP(6);
    const char* Wt = ws + WS_MT + (size_t)g * 5 * SLOT;
    if (w == 0) flags_wait(ws, WS_F2, 32, tag);
    wait_l_bar();
    STAMP(7);
#pragma unroll
    for (int st = 0; st < 4; ++st) tissue(Wt, st, smem + st * SLOT);
    f32x4 acc[5];
    gemm80<15, 10, 10, 5, 0>(smem, Wt, xa, acc, trb, [&](int) AINL {});
    STAMP(8);
    bsync();
#pragma unroll
    for (int t = 0; t < 5; ++t)
      *(u32x2*)(smem + (16 * w + r16) * 176 + (16 * t + 4 * h) * 2) = u32x2{pk_bf16(acc[t][0], acc[t][1]), pk_bf16(acc[t][2], acc[t][3])};
    bsync();
#pragma unroll
    for (int i = 0; i < 3; ++i) {
      const int id = tid + 256 * i;
      if (id < 640) {
        const int r = id / 10, c = id - r * 10;
        *(u32x4*)(out + (size_t)(rt * 64 + r) * (2 * DM) + 80 * g + c * 8) = *(const u32x4*)(smem + r * 176 + c * 16);
      }
    }
    STAMP(9);
    STAMP_FLUSH();
  }
}

DI void role_R(char* smem, int hd, const unsigned short* __restrict__ wo,
               char* __restrict__ ws) {
  const int tid = threadIdx.x, w = tid >> 6, lane = tid & 63, r16 = lane & 15, h = lane >> 4;
  const unsigned tag = __hip_atomic_load((const unsigned*)(ws + WS_EPOCH), __ATOMIC_RELAXED, __HIP_MEMORY_SCOPE_AGENT) + 1u;
  const int g = hd >> 2;
#pragma unroll
  for (int i = 0; i < 10; ++i) {
    const int c = tid + 256 * i;
    const int e = c / 80, cc = c - e * 80, g8 = cc / 10, ch = cc - g8 * 10;
    u32x4 v = {0u, 0u, 0u, 0u};
    if (e < 20) v = *(const u32x4*)(wo + (size_t)(20 * hd + e) * DM + cc * 8);
    *(u32x4*)(smem + g8 * 5120 + (e ^ (((e >> 3) & 1) << 2)) * 160 + ch * 16) = v;
  }
  *(u32x2*)(smem + 40960 + tid * 8) = u32x2{0u, 0u};
  bsync();
  const u64 want = flag_val(tag);
  if (w == 0) {
    const u64* pp = (const u64*)(ws + WS_KVP + ((size_t)(g * 32 + (lane & 31)) * 840 + (hd & 3) * 210 + 209) * 16 + 8);
    for (unsigned spins = 0; spins < (1u << 20); ++spins) {
      const u64 v = __hip_atomic_load(pp, __ATOMIC_RELAXED, __HIP_MEMORY_SCOPE_AGENT);
      if (__all(v == want)) break;
      __builtin_amdgcn_s_sleep(8);
    }
  }
  bsync();
  if (tid < 210) {
    const unsigned tlo = (unsigned)want, thi = (unsigned)(want >> 32);
    const char* p = ws + WS_KVP + ((size_t)g * 32 * 840 + (hd & 3) * 210 + tid) * 16;
    u32x4 v[32];
    for (unsigned spins = 0; spins < (1u << 20); ++spins) {
#pragma unroll
      for (int i = 0; i < 32; ++i)
        asm volatile("global_load_dwordx4 %0, %1, off sc1" : "=v"(v[i]) : "v"(p + (size_t)i * 840 * 16) : "memory");
      wait_v<0>();
      bool ok = true;
#pragma unroll
      for (int i = 0; i < 32; ++i) { asm volatile("" : "+v"(v[i])); ok = ok && v[i][2] == tlo && v[i][3] == thi; }
      if (__all(ok)) break;
      __builtin_amdgcn_s_sleep(4);
    }
    float s0 = 0.f, s1 = 0.f;
#pragma unroll
    for (int i = 0; i < 32; ++i) { s0 += __uint_as_float(v[i][0]); s1 += __uint_as_float(v[i][1]); }
    const unsigned pk = pk_bf16(s0, s1);
    const int e = tid / 10, d = 2 * (tid - e * 10);
    if (e == 20) {
      __hip_atomic_store((u64*)(ws + WS_Z) + hd * 10 + (d >> 1), ((u64)tag << 32) | (u64)pk, __ATOMIC_RELAXED, __HIP_MEMORY_SCOPE_AGENT);
    } else {
      unsigned short* kt = (unsigned short*)(smem + 40960);
      kt[d * 32 + e] = (unsigned short)(pk & 0xffff);
      kt[(d + 1) * 32 + e] = (unsigned short)(pk >> 16);
    }
  }
  bsync();
  {
    const int q = r16 >> 2, p = lane & 3;
    unsigned trb[2];
#pragma unroll
    for (int t = 0; t < 2; ++t) { int k = 8 * h + 4 * t + q; trb[t] = (k ^ (((k >> 3) & 1) << 2)) * 160 + p * 8; }
    s16x8 kb[2];
#pragma unroll
    for (int dt = 0; dt < 2; ++dt) kb[dt] = *(const s16x8*)(smem + 40960 + (16 * dt + r16) * 64 + h * 16);
    char* mst = smem + 45056;
#pragma unroll
    for (int nt = 0; nt < 10; ++nt) {
      const int g8 = 2 * w + nt / 5, t5 = nt % 5;
      const unsigned o = g8 * 5120 + t5 * 32;
      const s16x8 wf = cat8(tr_read(smem, o + trb[0]), tr_read(smem, o + trb[1]));
#pragma unroll
      for (int dt = 0; dt < 2; ++dt) {
        f32x4 dacc = {0.f, 0.f, 0.f, 0.f};
        dacc = mfma16(wf, kb[dt], dacc);
        const int d = 16 * dt + r16;
        if (d < 20) *(u32x2*)(mst + d * 1296 + (80 * g8 + 16 * t5 + 4 * h) * 2) = u32x2{pk_bf16(dacc[0], dacc[1]), pk_bf16(dacc[2], dacc[3])};
      }
    }
    bsync();
#pragma unroll
    for (int i = 0; i < 7; ++i) {
      const int c = tid + 256 * i;
      if (c < 1600) {
        const int d = c / 80, cc = c - d * 80, g8 = cc / 10, ch = cc - g8 * 10;
        const int f = 20 * hd + d, k = f & 63;
        st16_sc1(ws, WS_MT + (size_t)(g8 * 5 + (f >> 7)) * SLOT + ((f >> 6) & 1) * 10240 + (k ^ (((k >> 3) & 1) << 2)) * 160 + ch * 16,
                 *(const u32x4*)(mst + d * 1296 + cc * 16));
      }
    }
    wait_v<0>();
    wait_l_bar();
    if (tid == 0) flag_set(ws, WS_F2 + (size_t)hd * 128, tag);
  }
  if (hd == 0 && w == 0) {
    flags_wait(ws, WS_F2, 32, tag);
#pragma unroll 1
    for (int i = 0; i < 5; ++i) flags_wait(ws, WS_F4 + (size_t)i * 64 * 128, 64, tag);
    if (lane == 0) st4_sc1(ws + WS_EPOCH, tag);
  }
}

struct cplx { float r, i; };
DI cplx cmul(cplx a, cplx b) { return cplx{a.r * b.r - a.i * b.i, a.r * b.i + a.i * b.r}; }
DI cplx cexp_at(float ar, float ai, float p) {
  float m = __expf(ar * p);
  float x = ai * p;
  float k = rintf(x * 0.15915494309189535f);
  float r = fmaf(-k, 6.2831854820251465f, x);
  r = fmaf(-k, -1.7484556000744487e-07f, r);
  float rv = r * 0.15915494309189535f;
  return cplx{m * __builtin_amdgcn_cosf(rv), m * __builtin_amdgcn_sinf(rv)};
}
constexpr int KG_PITCH = 288;
constexpr int KG_WAVE = 57 * KG_PITCH;
constexpr int CV_KP = 2240;
constexpr int CV_U = 4 * CV_KP;
constexpr int CV_WAVE = CV_U + 65 * 80 + 48;
constexpr int B_T = 4 * KG_WAVE;
constexpr int B_RAW = 4 * CV_WAVE;
constexpr int B_T2 = B_RAW + 16384;

DI void role_B(char* smem, int bb, const unsigned short* __restrict__ u, const unsigned short* __restrict__ lar_,
               const unsigned short* __restrict__ aim_, const unsigned short* __restrict__ cr_,
               const unsigned short* __restrict__ ci_, const unsigned short* __restrict__ ldt_,
               const unsigned short* __restrict__ dsk_, char* __restrict__ ws, unsigned short* __restrict__ out) {
  const int tid = threadIdx.x, w = tid >> 6, lane = tid & 63, r16 = lane & 15, h4 = lane >> 4;
  const int d0 = ((bb & 7) * 20 + (bb >> 3)) * 4, d = d0 + w;
  STAMP_DECL
  STAMP(0);
  const unsigned tag = __hip_atomic_load((const unsigned*)(ws + WS_EPOCH), __ATOMIC_RELAXED, __HIP_MEMORY_SCOPE_AGENT) + 1u;
  {
    unsigned short* t_ = (unsigned short*)smem;
    u32x4 v[4];
#pragma unroll
    for (int i = 0; i < 4; ++i) {
      const int t = 2 * bb + (i >> 1), idx = tid + 256 * (i & 1), r = idx >> 3, c8 = idx & 7;
      v[i] = *(const u32x4*)(u + (size_t)((t / 10) * 64 + r) * DM + (t % 10) * 64 + c8 * 8);
    }
#pragma unroll
    for (int i = 0; i < 4; ++i) {
      const int idx = tid + 256 * (i & 1), r = idx >> 3, c8 = idx & 7;
#pragma unroll
      for (int j = 0; j < 4; ++j) *(unsigned*)(t_ + (i >> 1) * 4224 + r * 66 + c8 * 8 + 2 * j) = v[i][j];
    }
    bsync();
#pragma unroll
    for (int i = 0; i < 4; ++i) {
      const int t = 2 * bb + (i >> 1), idx = tid + 256 * (i & 1), c = idx >> 3, r8 = idx & 7;
      const unsigned short* tt = t_ + (i >> 1) * 4224;
      u32x4 o;
#pragma unroll
      for (int j = 0; j < 4; ++j) o[j] = (unsigned)tt[(r8 * 8 + 2 * j) * 66 + c] | ((unsigned)tt[(r8 * 8 + 2 * j + 1) * 66 + c] << 16);
      st16_sc1(ws, WS_UT + ((size_t)((t % 10) * 64 + c) * SEQ + (t / 10) * 64 + r8 * 8) * 2, o);
    }
    wait_l_bar();
  }
  STAMP(1);
  float inv_scale;
  {
    char* sm = smem + w * KG_WAVE;
    float lar[3], aim[3], crv[3], civ[3];
#pragma unroll
    for (int c = 0; c < 3; ++c) {
      const int n = d * NMODE + 64 * c + lane;
      lar[c] = bf2f(lar_[n]); aim[c] = bf2f(aim_[n]); crv[c] = bf2f(cr_[n]); civ[c] = bf2f(ci_[n]);
    }
    const float dt = bfr(expf(bf2f(ldt_[d])));
    float s = 0.f;
#pragma unroll
    for (int c = 0; c < 3; ++c) { crv[c] *= dt; civ[c] *= dt; s += fabsf(crv[c]) + fabsf(civ[c]); }
#pragma unroll
    for (int o = 32; o >= 1; o >>= 1) s += __shfl_xor(s, o);
    const float S = 2.f * s;
    int es = (int)((__float_as_uint(S) >> 23) & 0xff);
    int se = 268 - es; se = se < 1 ? 1 : (se > 253 ? 253 : se);
    const float sc = __uint_as_float((unsigned)se << 23);
    inv_scale = __uint_as_float((unsigned)(254 - se) << 23);
    f32x4 acc[3];
#pragma unroll
    for (int t = 0; t < 3; ++t) acc[t] = f32x4{0.f, 0.f, 0.f, 0.f};
    char* wp = sm + 4 * lane;
    const char* ap = sm + r16 * KG_PITCH + h4 * 16;
#pragma unroll 1
    for (int c = 0; c < DBG_NCH; ++c) {
      const float larc = c == 0 ? lar[0] : (c == 1 ? lar[1] : lar[2]);
      const float aimc = c == 0 ? aim[0] : (c == 1 ? aim[1] : aim[2]);
      const float crc = c == 0 ? crv[0] : (c == 1 ? crv[1] : crv[2]);
      const float cic = c == 0 ? civ[0] : (c == 1 ? civ[1] : civ[2]);
      const float a = bfr(expf(larc));
      const float ar = -a * dt, ai = aimc * dt;
      const cplx cc = cplx{crc * sc, cic * sc};
      auto putA = [&](int row, cplx v) AINL { *(unsigned*)(wp + row * KG_PITCH) = pk_f16(v.r, v.i); };
      auto putB = [&](int row, cplx v) AINL { *(unsigned*)(wp + (41 + row) * KG_PITCH) = pk_f16(v.r, v.i); };
      {
        cplx f[16];
        f[0] = cplx{1.f, 0.f}; f[1] = cexp_at(ar, -ai, 8.f);
        f[2] = cmul(f[1], f[1]); f[3] = cmul(f[2], f[1]); f[4] = cmul(f[2], f[2]);
        f[5] = cmul(f[4], f[1]); f[6] = cmul(f[4], f[2]); f[7] = cmul(f[4], f[3]); f[8] = cmul(f[4], f[4]);
#pragma unroll
        for (int j = 1; j < 8; ++j) f[8 + j] = cmul(f[8], f[j]);
#pragma unroll
        for (int j = 0; j < 16; ++j) putB(j, f[j]);
      }
      cplx e[8], g[8], cg[17];
      e[0] = cplx{1.f, 0.f}; e[1] = cexp_at(ar, ai, 1.f);
      e[2] = cmul(e[1], e[1]); e[3] = cmul(e[2], e[1]); e[4] = cmul(e[2], e[2]);
      e[5] = cmul(e[4], e[1]); e[6] = cmul(e[4], e[2]); e[7] = cmul(e[4], e[3]);
      g[0] = cplx{1.f, 0.f}; g[1] = cexp_at(ar, ai, 128.f);
      g[2] = cmul(g[1], g[1]); g[3] = cmul(g[2], g[1]); g[4] = cmul(g[2], g[2]);
      g[5] = cmul(g[4], g[1]); g[6] = cmul(g[4], g[2]); g[7] = cmul(g[4], g[3]);
      const cplx g8 = cexp_at(ar, ai, 1024.f);
      cg[0] = cc; cg[8] = cmul(cc, g8); cg[16] = cmul(cg[8], g8);
#pragma unroll
      for (int j = 1; j < 8; ++j) { cg[j] = cmul(cc, g[j]); cg[8 + j] = cmul(cg[8], g[j]); }
#pragma unroll
      for (int i = 0; i < 16; ++i) putA(i, (i & 7) ? cmul(cg[i >> 3], e[i & 7]) : cg[i >> 3]);
#pragma unroll
      for (int i = 0; i < 8; ++i) putA(16 + i, (i & 3) ? cmul(cg[2 + (i >> 2)], e[2 * (i & 3)]) : cg[2 + (i >> 2)]);
#pragma unroll
      for (int i = 0; i < 8; ++i) putA(24 + i, (i & 1) ? cmul(cg[4 + (i >> 1)], e[4]) : cg[4 + (i >> 1)]);
#pragma unroll
      for (int i = 0; i < 8; ++i) putA(32 + i, cg[8 + i]);
      putA(40, cg[16]);
      s16x8 bf[4], af[4][3];
#pragma unroll
      for (int ks = 0; ks < 4; ++ks) {
        bf[ks] = *(const s16x8*)(ap + 41 * KG_PITCH + ks * 64);
#pragma unroll
        for (int t = 0; t < 3; ++t) af[ks][t] = *(const s16x8*)(ap + 16 * t * KG_PITCH + ks * 64);
      }
#pragma unroll
      for (int ks = 0; ks < 4; ++ks)
#pragma unroll
        for (int t = 0; t < 3; ++t) acc[t] = mfma16h(af[ks][t], bf[ks], acc[t]);
      if (c == 0) {
        wait_v<0>();
        __builtin_amdgcn_s_barrier();
        if (tid < 2) { const int t = 2 * bb + tid; flag_set(ws, WS_F4 + (size_t)((t % 10) * 32 + t / 10) * 128, tag); }
      }
    }
    unsigned short* T = (unsigned short*)(smem + B_T2 + w * 1408);
#pragma unroll
    for (int t = 0; t < 3; ++t)
#pragma unroll
      for (int rg = 0; rg < 4; ++rg) {
        const int i = 4 * h4 + rg, j = r16;
        int idx;
        if (t == 0) idx = (i & 7) + 128 * (i >> 3) + 8 * j;
        else if (t == 1) idx = i < 8 ? 256 + (i & 3) + 64 * ((i >> 2) & 1) + 4 * j : 384 + (i & 1) + 32 * ((i >> 1) & 3) + 2 * j;
        else idx = i < 8 ? 512 + 16 * i + j : ((i == 8 && j == 0) ? 640 : 700);
        T[idx] = (unsigned short)(pk_f16(2.f * acc[t][rg], 0.f) & 0xffff);
      }
  }
  STAMP(2);
  bsync();
  flags_wait(ws, WS_F4 + (size_t)((d0 >> 6) * 32) * 128, 32, tag);
  STAMP(3);
  char* cb = smem + w * CV_WAVE;
  const unsigned coff = w * CV_WAVE;
  float uinv;
  {
    u32x4 uv[4];
    unsigned mx = 0;
#pragma unroll
    for (int c = 0; c < 4; ++c) uv[c] = *(const u32x4*)(ws + WS_UT + (size_t)d * SEQ * 2 + (lane + 64 * c) * 16);
    {
      const char* T = smem + B_T2 + w * 1408;
      char* k16 = smem + B_RAW + w * 4096;
#pragma unroll
      for (int c = 0; c < 4; ++c) {
        const int a = lane + 64 * c;
        u32x4 o;
        if (c == 0) {
          if (lane < 32) o = *(const u32x4*)(T + a * 16);
          else {
            const u32x2 e = *(const u32x2*)(T + 256 + a * 8);
            const unsigned e4 = *(const unsigned short*)(T + 256 + a * 8 + 8);
            o = u32x4{(e[0] & 0xffffu) * 0x10001u, (e[0] >> 16) | (e[1] << 16), (e[1] & 0xffffu) * 0x10001u, (e[1] >> 16) | (e4 << 16)};
          }
        } else if (c == 1) {
          const unsigned e = *(const unsigned*)(T + 512 + a * 4);
          const unsigned e2 = *(const unsigned short*)(T + 512 + a * 4 + 4);
          o = u32x4{(e & 0xffffu) * 0x10001u, e, (e >> 16) * 0x10001u, e2 * 0x10001u};
        } else {
          const unsigned e0 = *(const unsigned short*)(T + 768 + a * 2), e1 = *(const unsigned short*)(T + 770 + a * 2);
          o = u32x4{e0 * 0x10001u, e0 * 0x10001u, ((a & 1) ? e1 : e0) | (e1 << 16), e1 * 0x10001u};
        }
        *(u32x4*)(k16 + a * 16) = o;
      }
    }
#pragma unroll
    for (int c = 0; c < 4; ++c) {
#pragma unroll
      for (int j = 0; j < 4; ++j) mx = max(mx, max(uv[c][j] & 0x7fffu, (uv[c][j] >> 16) & 0x7fffu));
    }
#pragma unroll
    for (int o = 32; o >= 1; o >>= 1) mx = max(mx, (unsigned)__shfl_xor((int)mx, o));
    int eu = (int)(mx >> 7);
    int se = 268 - eu; se = se > 253 ? 253 : (se < 1 ? 1 : se);
    const float usc = __uint_as_float((unsigned)se << 23);
    uinv = __uint_as_float((unsigned)(254 - se) << 23);
#pragma unroll
    for (int c = 0; c < 4; ++c) {
      const int cidx = lane + 64 * c;
      u32x4 o4;
#pragma unroll
      for (int j = 0; j < 4; ++j) o4[j] = pk_f16(lo16(uv[c][j]) * usc, hi16(uv[c][j]) * usc);
      *(u32x4*)(cb + CV_U + ((cidx >> 2) + 1) * 80 + (cidx & 3) * 16) = o4;
    }
    if (lane < 5) *(u32x4*)(cb + CV_U + lane * 16) = u32x4{0u, 0u, 0u, 0u};
  }
  STAMP(4);
  const char* k16 = smem + B_RAW + w * 4096;
  const int i32 = lane & 31, h = lane >> 5, blk = (lane >> 4) & 1, q = (lane & 15) >> 2, p = lane & 3;
  const unsigned ab = coff + q * CV_KP + 2 * (32 + 16 * blk - 8 * h + 4 * p) - 40;
  const unsigned ubase = coff + CV_U + 16 * h;
  f32x16 acc0 = {}, acc1 = {};
#pragma unroll
  for (int pass = 0; pass < DBG_NPASS; ++pass) {
#pragma unroll
    for (int it = 0; it < 3; ++it) {
      const int qc = lane + 64 * it;
      if (qc < 136) {
        const int seg = qc >= 68;
        const int t0 = 1024 * seg + 512 * pass - 32 + 8 * (qc - 68 * seg);
        u32x4 a = {0u, 0u, 0u, 0u};
        u32x2 bb2 = {0u, 0u};
        if (t0 >= 0) a = *(const u32x4*)(k16 + t0 * 2);
        if (t0 >= 4) bb2 = *(const u32x2*)(k16 + t0 * 2 - 8);
        const unsigned s03 = __builtin_amdgcn_alignbit(a[0], bb2[1], 16), s01 = __builtin_amdgcn_alignbit(a[1], a[0], 16),
                       s12 = __builtin_amdgcn_alignbit(a[2], a[1], 16), s23 = __builtin_amdgcn_alignbit(a[3], a[2], 16),
                       sb = __builtin_amdgcn_alignbit(bb2[1], bb2[0], 16);
        char* dst = cb + 16 * qc;
        *(u32x4*)(dst) = a;
        *(u32x4*)(dst + CV_KP) = u32x4{s03, s01, s12, s23};
        *(u32x4*)(dst + 2 * CV_KP) = u32x4{opq(bb2[1]), opq(a[0]), opq(a[1]), opq(a[2])};
        *(u32x4*)(dst + 3 * CV_KP) = u32x4{sb, s03, s01, s12};
      }
    }
    s16x8 fa[4][8];
    auto ldfr = [&](int ml, s16x8 (&f)[8]) AINL {
      const int m = 16 * pass + ml;
      const unsigned a0 = ab + 64 * ml;
      const int r0 = i32 - m + 1;
      const unsigned u0 = ubase + 80 * (r0 > 0 ? r0 : 0);
      const unsigned u1 = ubase + 80 * (i32 - m + 33);
#pragma unroll
      for (int ks = 0; ks < 2; ++ks) {
        const unsigned ao = a0 + 32 * (1 - ks);
        f[ks] = cat8(tr_read(smem, ao + 8), tr_read(smem, ao));
        f[2 + ks] = cat8(tr_read(smem, ao + 1088 + 8), tr_read(smem, ao + 1088));
        f[4 + ks] = *(const s16x8*)(smem + u0 + 32 * ks);
        f[6 + ks] = *(const s16x8*)(smem + u1 + 32 * ks);
      }
    };
    auto mm = [&](s16x8 (&f)[8]) AINL {
#pragma unroll
      for (int ks = 0; ks < 2; ++ks) {
        acc0 = mfma32h(f[ks], f[4 + ks], acc0);
        acc1 = mfma32h(f[ks], f[6 + ks], acc1);
        acc1 = mfma32h(f[2 + ks], f[4 + ks], acc1);
      }
    };
    STAMP(5 + 2 * pass);
    ldfr(0, fa[0]);
    ldfr(1, fa[1]);
#pragma unroll 1
    for (int m = 0; m < 16; m += 4) {
      ldfr(m + 2, fa[2]);
      __builtin_amdgcn_sched_barrier(0);
      mm(fa[0]);
      __builtin_amdgcn_sched_barrier(0);
      ldfr(m + 3, fa[3]);
      __builtin_amdgcn_sched_barrier(0);
      mm(fa[1]);
      __builtin_amdgcn_sched_barrier(0);
      ldfr((m + 4) & 15, fa[0]);
      __builtin_amdgcn_sched_barrier(0);
      mm(fa[2]);
      __builtin_amdgcn_sched_barrier(0);
      ldfr((m + 5) & 15, fa[1]);
      __builtin_amdgcn_sched_barrier(0);
      mm(fa[3]);
      __builtin_amdgcn_sched_barrier(0);
    }
    STAMP(6 + 2 * pass);
  }
  {
    const float inv = inv_scale * uinv;
    const float dk = bf2f(dsk_[d]);
    char* raw = smem + B_RAW + w * 4096;
#pragma unroll
    for (int c = 0; c < 2; ++c)
#pragma unroll
      for (int g4 = 0; g4 < 4; ++g4) {
        const int l = 1024 * c + 32 * i32 + 8 * g4 + 4 * h;
        const u32x2 uu = *(const u32x2*)(cb + CV_U + ((l >> 5) + 1) * 80 + (l & 31) * 2);
        float r[4];
#pragma unroll
        for (int j = 0; j < 4; ++j) {
          const float y = bfr((c ? acc1[4 * g4 + j] : acc0[4 * g4 + j]) * inv);
          const unsigned short hb = (unsigned short)((j & 1) ? (uu[j >> 1] >> 16) : (uu[j >> 1] & 0xffffu));
          const float uf = (float)__builtin_bit_cast(_Float16, hb) * uinv;
          r[j] = y + bfr(uf * dk);
        }
        *(u32x2*)(raw + l * 2) = u32x2{pk_bf16(r[0], r[1]), pk_bf16(r[2], r[3])};
      }
  }
  bsync();
  {
    const unsigned short* rt_ = (const unsigned short*)(smem + B_RAW);
#pragma unroll
    for (int i = 0; i < 8; ++i) {
      const int l = tid + 256 * i;
      const u32x2 v = {(unsigned)rt_[l] | ((unsigned)rt_[2048 + l] << 16), (unsigned)rt_[4096 + l] | ((unsigned)rt_[6144 + l] << 16)};
      *(u32x2*)(out + (size_t)l * (2 * DM) + DM + d0) = v;
    }
  }
  STAMP(9);
}

__global__ NOPK __launch_bounds__(256, 2) void k_fused(
    const unsigned short* __restrict__ x, const unsigned short* __restrict__ wqkv, const unsigned short* __restrict__ wo,
    const unsigned short* __restrict__ u, const unsigned short* __restrict__ lar, const unsigned short* __restrict__ aim,
    const unsigned short* __restrict__ cr, const unsigned short* __restrict__ ci, const unsigned short* __restrict__ ldt,
    const unsigned short* __restrict__ dsk, char* __restrict__ ws, unsigned short* __restrict__ out) {
  extern __shared__ __attribute__((aligned(16))) char smem[];
  const int b = blockIdx.x;
  if (b < NB_A) role_A(smem, b, x, wqkv, wo, ws, out);
  else if (b < NB_A + NB_R) role_R(smem, b - NB_A, wo, ws);
  else {
    role_B(smem, b - NB_A - NB_R, u, lar, aim, cr, ci, ldt, dsk, ws, out);
  }
}

extern "C" void kernel_launch(void* const* d_in, const int* in_sizes, int n_in, void* d_out, int out_size, void* d_ws,
                              size_t ws_size, hipStream_t stream) {
  constexpr int LDS = 81920;
  (void)hipFuncSetAttribute((const void*)k_fused, hipFuncAttributeMaxDynamicSharedMemorySize, LDS);
  k_fused<<<NB_A + NB_R + NB_B, 256, LDS, stream>>>(
      (const unsigned short*)d_in[0], (const unsigned short*)d_in[1], (const unsigned short*)d_in[2],
      (const unsigned short*)d_in[3], (const unsigned short*)d_in[4], (const unsigned short*)d_in[5],
      (const unsigned short*)d_in[6], (const unsigned short*)d_in[7], (const unsigned short*)d_in[8],
      (const unsigned short*)d_in[9], (char*)d_ws, (unsigned short*)d_out);
}
```

```cpp
#include <hip/hip_runtime.h>
#include <hip/hip_bf16.h>
#include <stdint.h>

#define DI __device__ __forceinline__
#define NOPK __attribute__((target("no-packed-fp32-ops")))
#define AINL __attribute__((always_inline))
#define LDS_AS __attribute__((address_space(3)))
#define GLB_AS __attribute__((address_space(1)))

typedef __attribute__((ext_vector_type(8))) short s16x8;
typedef __attribute__((ext_vector_type(4))) short s16x4;
typedef __attribute__((ext_vector_type(8))) _Float16 f16x8;
typedef __attribute__((ext_vector_type(2))) _Float16 f16x2;
typedef __attribute__((ext_vector_type(8))) __bf16 b16x8;
typedef __attribute__((ext_vector_type(2))) __bf16 b16x2;
typedef __attribute__((ext_vector_type(4))) float f32x4;
typedef __attribute__((ext_vector_type(2))) float f32x2;
typedef __attribute__((ext_vector_type(16))) float f32x16;
typedef __attribute__((ext_vector_type(4))) unsigned u32x4;
typedef __attribute__((ext_vector_type(2))) unsigned u32x2;
typedef unsigned long long u64;

#define DBG_NPASS 2
#define DBG_NCH 3
constexpr int SEQ = 2048, DM = 640, NMODE = 192;
constexpr int NB_A = 256, NB_R = 64, NB_B = 160;

constexpr size_t WS_EPOCH = 0;
constexpr size_t WS_F2  = 4096;
constexpr size_t WS_F3  = WS_F2 + 64 * 128;
constexpr size_t WS_F4  = WS_F3 + 256 * 128;
constexpr size_t WS_KVP = WS_F4 + 320 * 128;
constexpr size_t WS_Z   = WS_KVP + (size_t)8 * 32 * 840 * 16;
constexpr size_t WS_PQ  = WS_Z + 4096;
constexpr size_t WS_MT  = WS_PQ + (size_t)SEQ * DM * 2;
constexpr size_t WS_UT  = WS_MT + (size_t)DM * DM * 2;

#define STAMP(i) do { } while (0)
#define STAMP_DECL
#define STAMP_FLUSH() do { } while (0)
template <int V> struct IC { static constexpr int value = V; };
template <int N, int I = 0, class F> DI void static_for(F& f) {
  if constexpr (I < N) { f(IC<I>{}); static_for<N, I + 1>(f); }
}
DI float bf2f(unsigned short h) { return __uint_as_float((unsigned)h << 16); }
DI unsigned pk_bf16(float a, float b) { f32x2 v = {a, b}; return __builtin_bit_cast(unsigned, __builtin_convertvector(v, b16x2)); }
DI unsigned pk_f16(float a, float b) { f32x2 v = {a, b}; return __builtin_bit_cast(unsigned, __builtin_convertvector(v, f16x2)); }
DI float bfr(float x) { return __uint_as_float(pk_bf16(x, 0.f) << 16); }
DI float lo16(unsigned p) { return __uint_as_float(p << 16); }
DI float hi16(unsigned p) { return __uint_as_float(p & 0xffff0000u); }
DI unsigned opq(unsigned x) { asm("" : "+v"(x)); return x; }

DI f32x4 mfma16(s16x8 a, s16x8 b, f32x4 c) {
  return __builtin_amdgcn_mfma_f32_16x16x32_bf16(__builtin_bit_cast(b16x8, a), __builtin_bit_cast(b16x8, b), c, 0, 0, 0);
}
DI f32x4 mfma16h(s16x8 a, s16x8 b, f32x4 c) {
  return __builtin_amdgcn_mfma_f32_16x16x32_f16(__builtin_bit_cast(f16x8, a), __builtin_bit_cast(f16x8, b), c, 0, 0, 0);
}
DI f32x16 mfma32h(s16x8 a, s16x8 b, f32x16 c) {
  return __builtin_amdgcn_mfma_f32_32x32x16_f16(__builtin_bit_cast(f16x8, a), __builtin_bit_cast(f16x8, b), c, 0, 0, 0);
}
DI s16x4 tr_read(const char* smem, unsigned off) {
  return __builtin_amdgcn_ds_read_tr16_b64_v4i16((LDS_AS s16x4*)(smem + off));
}
DI s16x8 cat8(s16x4 a, s16x4 b) { return __builtin_shufflevector(a, b, 0, 1, 2, 3, 4, 5, 6, 7); }

DI void bsync() {
  __builtin_amdgcn_fence(__ATOMIC_RELEASE, "workgroup");
  __builtin_amdgcn_s_barrier();
  __builtin_amdgcn_fence(__ATOMIC_ACQUIRE, "workgroup");
}
template <int N> DI void wait_v() { asm volatile("s_waitcnt vmcnt(%0)" ::"n"(N) : "memory"); }
DI void wait_l_bar() { asm volatile("s_waitcnt lgkmcnt(0)" ::: "memory"); __builtin_amdgcn_s_barrier(); }
DI void glds16(const void* g, char* l) {
  __builtin_amdgcn_global_load_lds((const GLB_AS unsigned*)g, (LDS_AS unsigned*)l, 16, 0, 0);
}
DI void glds16_sc1(const void* g, char* l) { glds16(g, l); }
DI void st16_sc1(char* ws, size_t off, u32x4 v) {
  __builtin_amdgcn_raw_buffer_store_b128(v, __builtin_amdgcn_make_buffer_rsrc(ws, 0, 64u << 20, 0x00020000), (int)off, 0, 16);
}
DI void st4_sc1(void* p, unsigned v) { __hip_atomic_store((unsigned*)p, v, __ATOMIC_RELAXED, __HIP_MEMORY_SCOPE_AGENT); }
DI u64 flag_val(unsigned tag) { return ((u64)tag << 32) | (u64)(tag ^ 0x9e3779b9u); }
DI void flag_set(char* ws, size_t off, unsigned tag) {
  __hip_atomic_store((u64*)(ws + off), flag_val(tag), __ATOMIC_RELAXED, __HIP_MEMORY_SCOPE_AGENT);
}
DI void flags_wait(const char* ws, size_t off, int n, unsigned tag) {
  const int lane = threadIdx.x & 63;
  const u64 want = flag_val(tag);
  const u64* p = (const u64*)(ws + off + (size_t)(lane < n ? lane : 0) * 128);
  for (unsigned spins = 0; spins < (1u << 20); ++spins) {
    const u64 v = __hip_atomic_load(p, __ATOMIC_RELAXED, __HIP_MEMORY_SCOPE_AGENT);
    if (__all(v == want)) break;
    __builtin_amdgcn_s_sleep(6);
  }
}

DI float phi(float t) {
  float a = bfr(t + 1.0f);
  float e = bfr(__expf(t));
  float s = t > 0.f ? a : e;
  return bfr(s + 1e-6f);
}

constexpr int SLOT = 20480;
constexpr int XS = 3 * SLOT;

DI void ilv5() {
#pragma unroll
  for (int i = 0; i < 5; ++i) { __builtin_amdgcn_sched_group_barrier(0x008, 1, 0); __builtin_amdgcn_sched_group_barrier(0x100, 2, 0); }
}
struct WSrc { unsigned off[5]; };
DI void wsrc_init(WSrc& s, int ldw, int colA, int colB, int kB) {
  const int w = threadIdx.x >> 6, lane = threadIdx.x & 63;
#pragma unroll
  for (int i = 0; i < 5; ++i) {
    int c = (w + 4 * i) * 64 + lane;
    int unit = c >= 640, cu = c - unit * 640;
    int pos = cu / 10, ch = cu - pos * 10;
    int k = pos ^ (((pos >> 3) & 1) << 2);
    s.off[i] = (unsigned)((k + (unit ? kB : 0)) * ldw + (unit ? colB : colA) + ch * 8) * 2u;
  }
}
DI void wissue(const WSrc& s, const char* Wst, char* slot) {
  char* dst = slot + (threadIdx.x >> 6) * 1024;
#pragma unroll
  for (int i = 0; i < 5; ++i) glds16(Wst + s.off[i], dst + i * 4096);
}

DI void tissue(const char* Wt, int st, char* slot) {
  const int w = threadIdx.x >> 6, lane = threadIdx.x & 63;
#pragma unroll
  for (int i = 0; i < 5; ++i) glds16_sc1(Wt + st * SLOT + (w + 4 * i) * 1024 + lane * 16, slot + (w + 4 * i) * 1024);
}
template <int V0, int V1, int V2, int V3, int V4, class HOOK>
DI void gemm80(char* smem, const char* Wt, s16x8 (&xa)[20], f32x4 (&acc)[5], const unsigned (&trb)[2], HOOK hook) {
  const int w = threadIdx.x >> 6, lane = threadIdx.x & 63;
#pragma unroll
  for (int t = 0; t < 5; ++t) acc[t] = f32x4{0.f, 0.f, 0.f, 0.f};
#pragma unroll
  for (int st = 0; st < 5; ++st) {
    if (st == 0) wait_v<V0>(); else if (st == 1) wait_v<V1>(); else if (st == 2) wait_v<V2>();
    else if (st == 3) wait_v<V3>(); else wait_v<V4>();
    wait_l_bar();
    hook(st);
    const unsigned sb = (st & 3) * SLOT;
    s16x8 wf[2][5];
#pragma unroll
    for (int t = 0; t < 5; ++t) wf[0][t] = cat8(tr_read(smem, sb + t * 32 + trb[0]), tr_read(smem, sb + t * 32 + trb[1]));
#pragma unroll
    for (int kk = 0; kk < 4; ++kk) {
      if (kk < 3) {
        const unsigned o = sb + ((kk + 1) >> 1) * 10240 + ((kk + 1) & 1) * 5120;
#pragma unroll
        for (int t = 0; t < 5; ++t) wf[(kk + 1) & 1][t] = cat8(tr_read(smem, o + t * 32 + trb[0]), tr_read(smem, o + t * 32 + trb[1]));
      }
#pragma unroll
      for (int t = 0; t < 5; ++t) acc[t] = mfma16(wf[kk & 1][t], xa[st * 4 + kk], acc[t]);
      if (st == 1) {
        glds16_sc1(Wt + 4 * SLOT + (w + 4 * kk) * 1024 + lane * 16, smem + (w + 4 * kk) * 1024);
        if (kk == 3) glds16_sc1(Wt + 4 * SLOT + (w + 16) * 1024 + lane * 16, smem + (w + 16) * 1024);
      }
      ilv5();
      __builtin_amdgcn_sched_barrier(0);
    }
  }
}

constexpr int NIT = 19;
DI constexpr int item_typ(int it) { return it >= 14 ? 1 : ((it == 0 || it == 3 || it == 7 || it == 10) ? 0 : 2); }
DI constexpr int item_idx(int it) {
  return it >= 14 ? it - 14 : (it == 0 ? 0 : it == 3 ? 1 : it == 7 ? 2 : it == 10 ? 3 : it < 3 ? it - 1 : it < 7 ? it - 2 : it < 10 ? it - 3 : it - 4);
}
DI void role_A(char* smem, int b, const unsigned short* __restrict__ x, const unsigned short* __restrict__ wqkv,
               const unsigned short* __restrict__ wo, char* __restrict__ ws, unsigned short* __restrict__ out) {
  const int tid = threadIdx.x, w = tid >> 6, lane = tid & 63, r16 = lane & 15, h = lane >> 4;
  const int xcd = b & 7, idx = b >> 3;
  const int g = (xcd & 1) * 4 + (idx & 3), rt = (xcd >> 1) * 8 + (idx >> 2);
  STAMP_DECL
  STAMP(0);
  const unsigned tag = __hip_atomic_load((const unsigned*)(ws + WS_EPOCH), __ATOMIC_RELAXED, __HIP_MEMORY_SCOPE_AGENT) + 1u;
  unsigned trb[2];
  {
    const int q = r16 >> 2, p = lane & 3;
#pragma unroll
    for (int t = 0; t < 2; ++t) { int k = 8 * h + 4 * t + q; trb[t] = (k ^ (((k >> 3) & 1) << 2)) * 160 + p * 8; }
  }
  s16x8 xa[20];
  const u64 want = flag_val(tag);
  {
    WSrc wq_, wk_;
    wsrc_init(wq_, 3 * DM, 80 * g, 80 * g, 64);
    wsrc_init(wk_, 3 * DM, DM + 80 * g, 2 * DM + 80 * g, 0);
    const char* Wb = (const char*)wqkv;
    unsigned xoff[5];
#pragma unroll
    for (int i = 0; i < 5; ++i) {
      const int c = (w + 4 * i) * 64 + lane, row = c / 20, ch = c - row * 20;
      xoff[i] = (unsigned)(rt * 64 + row) * (DM * 2) + ch * 16;
    }
    auto piece = [&](int it, int i) AINL {
      char* dst = smem + (it & 3) * SLOT + w * 1024 + i * 4096;
      if (item_typ(it) == 0) glds16((const char*)x + xoff[i] + item_idx(it) * 320, dst);
      else if (item_typ(it) == 1) glds16(Wb + (size_t)item_idx(it) * (128u * 3 * DM * 2) + wq_.off[i], dst);
      else glds16(Wb + (size_t)item_idx(it) * (64u * 3 * DM * 2) + wk_.off[i], dst);
    };
#pragma unroll
    for (int it = 0; it < 3; ++it)
#pragma unroll
      for (int i = 0; i < 5; ++i) piece(it, i);
    f32x4 acc[10];
#pragma unroll
    for (int t = 0; t < 10; ++t) acc[t] = f32x4{0.f, 0.f, 0.f, 0.f};
    const unsigned xrd = (16 * w + r16) * 320 + h * 16;
    const auto rsrc = __builtin_amdgcn_make_buffer_rsrc(ws, 0, 64u << 20, 0x00020000);
    auto step = [&](auto itc) AINL {
      constexpr int it = decltype(itc)::value;
      if (it <= 13) wait_v<10>(); else if (it <= 16) wait_v<20>(); else if (it == 17) wait_v<5>(); else wait_v<0>();
      wait_l_bar();
      if (it == 0) STAMP(1);
      const unsigned sb = (it & 3) * SLOT;
      if constexpr (item_typ(it) == 0) {
#pragma unroll
        for (int kk = 0; kk < 5; ++kk) xa[5 * item_idx(it) + kk] = *(const s16x8*)(smem + sb + xrd + kk * 64);
#pragma unroll
        for (int i = 0; i < 5; ++i) piece(it + 3, i);
      } else if constexpr (item_typ(it) == 1) {
        constexpr int st = item_idx(it);
        s16x8 wf[2][5];
#pragma unroll
        for (int t = 0; t < 5; ++t) wf[0][t] = cat8(tr_read(smem, sb + t * 32 + trb[0]), tr_read(smem, sb + t * 32 + trb[1]));
#pragma unroll
        for (int kk = 0; kk < 4; ++kk) {
          if (kk < 3) {
            const unsigned o = sb + ((kk + 1) >> 1) * 10240 + ((kk + 1) & 1) * 5120;
#pragma unroll
            for (int t = 0; t < 5; ++t) wf[(kk + 1) & 1][t] = cat8(tr_read(smem, o + t * 32 + trb[0]), tr_read(smem, o + t * 32 + trb[1]));
          }
#pragma unroll
          for (int t = 0; t < 5; ++t) acc[t] = mfma16(wf[kk & 1][t], xa[st * 4 + kk], acc[t]);
          if (it + 3 < NIT) { piece(it + 3, kk); if (kk == 3) piece(it + 3, 4); }
          ilv5();
          __builtin_amdgcn_sched_barrier(0);
        }
      } else {
        constexpr int st = item_idx(it);
        s16x8 wf[2][5];
#pragma unroll
        for (int t = 0; t < 5; ++t) wf[0][t] = cat8(tr_read(smem, sb + t * 32 + trb[0]), tr_read(smem, sb + t * 32 + trb[1]));
#pragma unroll
        for (int j = 0; j < 4; ++j) {
          if (j < 3) {
            const unsigned o = sb + ((j + 1) & 1) * 10240 + ((j + 1) >> 1) * 5120;
#pragma unroll
            for (int t = 0; t < 5; ++t) wf[(j + 1) & 1][t] = cat8(tr_read(smem, o + t * 32 + trb[0]), tr_read(smem, o + t * 32 + trb[1]));
          }
#pragma unroll
          for (int t = 0; t < 5; ++t) acc[(j & 1) * 5 + t] = mfma16(xa[2 * st + (j >> 1)], wf[j & 1][t], acc[(j & 1) * 5 + t]);
          piece(it + 3, j); if (j == 3) piece(it + 3, 4);
          ilv5();
          __builtin_amdgcn_sched_barrier(0);
        }
      }
      if constexpr (it == 13) {
        STAMP(2);
        char* im = smem + sb;
        wait_l_bar();
#pragma unroll
        for (int t = 0; t < 10; ++t) {
          float v0 = bfr(acc[t][0]), v1 = bfr(acc[t][1]), v2 = bfr(acc[t][2]), v3 = bfr(acc[t][3]);
          if (t < 5) { v0 = phi(v0); v1 = phi(v1); v2 = phi(v2); v3 = phi(v3); }
          *(u32x2*)(im + (16 * t + r16) * 128 + (((2 * w + (h >> 1)) ^ (r16 & 7)) << 4) + (h & 1) * 8) = u32x2{pk_bf16(v0, v1), pk_bf16(v2, v3)};
          acc[t] = f32x4{0.f, 0.f, 0.f, 0.f};
        }
        wait_l_bar();
        const unsigned outp = (unsigned)(WS_KVP + (size_t)(g * 32 + rt) * 840 * 16);
        const unsigned tlo = (unsigned)want, thi = (unsigned)(want >> 32);
        s16x8 ones;
#pragma unroll
        for (int i = 0; i < 8; ++i) ones[i] = (short)0x3f80;
#pragma unroll
        for (int jj = 0; jj < 5; ++jj) {
          const int j = 4 * jj + w;
          const int kt = j < 13 ? (j + 1 + (j >= 2) + 0) / 3 + (j >= 13 ? 0 : 0) : (j < 18 ? j - 13 : 0);
          const int ktb = (j + 1) / 3, vtb = j - 2 * ktb - (ktb > 0 ? 0 : 0);
          const int kq = j < 13 ? ktb : (j < 18 ? j - 13 : 0);
          const int vq = j < 13 ? (j + 1) - 3 * ktb + ktb - 1 : -1;
          (void)kt; (void)vtb;
          f32x4 dacc = {0.f, 0.f, 0.f, 0.f};
#pragma unroll
          for (int ks = 0; ks < 2; ++ks) {
            const unsigned co = ((4 * ks + h) ^ (r16 & 7)) << 4;
            s16x8 af = *(const s16x8*)(im + (16 * kq + r16) * 128 + co);
            s16x8 bf = *(const s16x8*)(im + (80 + 16 * (vq < 0 ? 0 : vq) + r16) * 128 + co);
            dacc = mfma16(af, vq >= 0 ? bf : ones, dacc);
          }
          const int kf = 16 * kq + 4 * h, hk = kf / 20, dd = kf - 20 * hk;
          const int vf = 16 * vq + r16, hv = vq >= 0 ? vf / 20 : hk, e = vq >= 0 ? vf - 20 * hv : 20;
          const bool ok = j < 18 && hk == hv && (vq >= 0 || r16 == 0);
          const unsigned o = ok ? outp + (unsigned)((hk * 21 + e) * 10 + (dd >> 1)) * 16u : 0x7ff00000u;
          __builtin_amdgcn_raw_buffer_store_b128(u32x4{__float_as_uint(dacc[0]), __float_as_uint(dacc[1]), tlo, thi}, rsrc, (int)o, 0, 16);
          __builtin_amdgcn_raw_buffer_store_b128(u32x4{__float_as_uint(dacc[2]), __float_as_uint(dacc[3]), tlo, thi}, rsrc, (int)(o + 16u), 0, 16);
        }
        __builtin_amdgcn_sched_barrier(0);
        STAMP(3);
      }
    };
    static_for<NIT>(step);
#pragma unroll
    for (int t = 0; t < 5; ++t) {
      const int f = 80 * g + 16 * t + 4 * h, f8 = f >> 3;
      const u32x2 v = {pk_bf16(phi(bfr(acc[t][0])), phi(bfr(acc[t][1]))), pk_bf16(phi(bfr(acc[t][2])), phi(bfr(acc[t][3])))};
      __builtin_amdgcn_raw_buffer_store_b64(v, rsrc,
          (int)(WS_PQ + ((size_t)((rt * 4 + w) * 20 + (f8 >> 2)) * 1024 + ((f8 & 3) * 16 + r16) * 16 + (f & 4) * 2)), 0, 16);
    }
    wait_v<0>();
    wait_l_bar();
    if (tid == 0) flag_set(ws, WS_F3 + (size_t)(rt * 8 + g) * 128, tag);
    STAMP(4);
    if (w == 0) flags_wait(ws, WS_F3 + (size_t)(rt * 8) * 128, 8, tag);
    __builtin_amdgcn_s_barrier();
    {
      const char* op = ws + WS_PQ + (size_t)(rt * 4 + w) * 20 * 1024 + lane * 16;
#pragma unroll
      for (int ks = 0; ks < 20; ++ks)
        asm volatile("global_load_dwordx4 %0, %1, off offset:%2" : "=v"(xa[ks]) : "v"(op + (ks >> 2) * 4096), "i"((ks & 3) * 1024) : "memory");
    }
  }
  {
    wait_l_bar();
    {
      const u64* gp = (const u64*)(ws + WS_Z);
      u64 v0, v1;
      if (w == 0) {
        for (unsigned spins = 0; spins < (1u << 20); ++spins) {
          const u64 v = __hip_atomic_load(gp + (lane & 31) * 10 + 9, __ATOMIC_RELAXED, __HIP_MEMORY_SCOPE_AGENT);
          if (__all((unsigned)(v >> 32) == tag)) break;
          __builtin_amdgcn_s_sleep(6);
        }
      }
      __builtin_amdgcn_s_barrier();
      for (unsigned spins = 0; spins < (1u << 20); ++spins) {
        v0 = __hip_atomic_load(gp + tid, __ATOMIC_RELAXED, __HIP_MEMORY_SCOPE_AGENT);
        v1 = __hip_atomic_load(gp + 256 + (tid & 63), __ATOMIC_RELAXED, __HIP_MEMORY_SCOPE_AGENT);
        if (__all((unsigned)(v0 >> 32) == tag && (unsigned)(v1 >> 32) == tag)) break;
        __builtin_amdgcn_s_sleep(1);
      }
      ((unsigned*)smem)[tid] = (unsigned)v0;
      if (tid < 64) ((unsigned*)smem)[256 + tid] = (unsigned)v1;
    }
    wait_v<0>();
#pragma unroll
    for (int ks = 0; ks < 20; ++ks) asm volatile("" : "+v"(xa[ks]));
    bsync();
    STAMP(5);
    float* rcp = (float*)(smem + 2048 + w * 2176);
#pragma unroll
    for (int T = 0; T < 2; ++T) {
      f32x4 dacc = {0.f, 0.f, 0.f, 0.f};
      const int fbase = 20 * (16 * T + r16);
#pragma unroll
      for (int k2 = 0; k2 < 10; ++k2) {
        const int ks = 10 * T + k2, f0 = 32 * ks + 8 * h;
        u32x4 zz = *(const u32x4*)(smem + f0 * 2);
#pragma unroll
        for (int j = 0; j < 4; ++j) {
          const unsigned d0 = (unsigned)(f0 + 2 * j - fbase);
          zz[j] = d0 < 20u ? zz[j] : 0u;
        }
        dacc = mfma16(__builtin_bit_cast(s16x8, zz), xa[ks], dacc);
      }
#pragma unroll
      for (int rg = 0; rg < 4; ++rg) rcp[r16 * 34 + 16 * T + 4 * h + rg] = 1.0f / bfr(bfr(dacc[rg]) + 1e-6f);
    }
#pragma unroll
    for (int ks = 0; ks < 20; ++ks) {
      const int f0 = 32 * ks + 8 * h;
      u32x4 v = __builtin_bit_cast(u32x4, xa[ks]);
#pragma unroll
      for (int j = 0; j < 4; ++j) {
        const float rc = rcp[r16 * 34 + (f0 + 2 * j) / 20];
        v[j] = pk_bf16(lo16(v[j]) * rc, hi16(v[j]) * rc);
      }
      xa[ks] = __builtin_bit_cast(s16x8, v);
    }
    STAMP(6);
    const char* Wt = ws + WS_MT + (size_t)g * 5 * SLOT;
    if (w == 0) flags_wait(ws, WS_F2, 64, tag);
    wait_l_bar();
    STAMP(7);
#pragma unroll
    for (int st = 0; st < 4; ++st) tissue(Wt, st, smem + st * SLOT);
    f32x4 acc[5];
    gemm80<15, 10, 10, 5, 0>(smem, Wt, xa, acc, trb, [&](int) AINL {});
    STAMP(8);
    bsync();
#pragma unroll
    for (int t = 0; t < 5; ++t)
      *(u32x2*)(smem + (16 * w + r16) * 176 + (16 * t + 4 * h) * 2) = u32x2{pk_bf16(acc[t][0], acc[t][1]), pk_bf16(acc[t][2], acc[t][3])};
    bsync();
#pragma unroll
    for (int i = 0; i < 3; ++i) {
      const int id = tid + 256 * i;
      if (id < 640) {
        const int r = id / 10, c = id - r * 10;
        *(u32x4*)(out + (size_t)(rt * 64 + r) * (2 * DM) + 80 * g + c * 8) = *(const u32x4*)(smem + r * 176 + c * 16);
      }
    }
    STAMP(9);
    STAMP_FLUSH();
  }
}

DI void role_R(char* smem, int rb, const unsigned short* __restrict__ wo, char* __restrict__ ws) {
  const int tid = threadIdx.x, w = tid >> 6, lane = tid & 63, r16 = lane & 15, h = lane >> 4;
  const unsigned tag = __hip_atomic_load((const unsigned*)(ws + WS_EPOCH), __ATOMIC_RELAXED, __HIP_MEMORY_SCOPE_AGENT) + 1u;
  const int hd = rb >> 1, half = rb & 1;
  const int g = hd >> 2;
  STAMP_DECL
  STAMP(0);
#pragma unroll
  for (int i = 0; i < 10; ++i) {
    const int c = tid + 256 * i;
    const int e = c / 80, cc = c - e * 80, g8 = cc / 10, ch = cc - g8 * 10;
    u32x4 v = {0u, 0u, 0u, 0u};
    if (e < 20) v = *(const u32x4*)(wo + (size_t)(20 * hd + e) * DM + cc * 8);
    *(u32x4*)(smem + g8 * 5120 + (e ^ (((e >> 3) & 1) << 2)) * 160 + ch * 16) = v;
  }
  if (tid < 128) *(u32x2*)(smem + 40960 + tid * 8) = u32x2{0u, 0u};
  const u64 want = flag_val(tag);
  if (w == 0) {
    const u64* pp = (const u64*)(ws + WS_KVP + ((size_t)(g * 32 + (lane & 31)) * 840 + (hd & 3) * 210 + 200 + 5 * half) * 16 + 8);
    for (unsigned spins = 0; spins < (1u << 20); ++spins) {
      const u64 v = __hip_atomic_load(pp, __ATOMIC_RELAXED, __HIP_MEMORY_SCOPE_AGENT);
      if (__all(v == want)) break;
      __builtin_amdgcn_s_sleep(6);
    }
  }
  bsync();
  STAMP(1);
  {
    const int gi = tid >> 1, ph = tid & 1;
    const int e = (gi < 105 ? gi : 104) / 5, dp = (gi < 105 ? gi : 104) - 5 * e;
    const unsigned tlo = (unsigned)want, thi = (unsigned)(want >> 32);
    const char* p = ws + WS_KVP + ((size_t)(g * 32 + 16 * ph) * 840 + (hd & 3) * 210 + e * 10 + 5 * half + dp) * 16;
    u32x4 v[16];
    for (unsigned spins = 0; spins < (1u << 20); ++spins) {
#pragma unroll
      for (int i = 0; i < 16; ++i)
        asm volatile("global_load_dwordx4 %0, %1, off sc1" : "=v"(v[i]) : "v"(p + (size_t)i * 840 * 16) : "memory");
      wait_v<0>();
      bool ok = true;
#pragma unroll
      for (int i = 0; i < 16; ++i) { asm volatile("" : "+v"(v[i])); ok = ok && v[i][2] == tlo && v[i][3] == thi; }
      if (__all(ok)) break;
      __builtin_amdgcn_s_sleep(4);
    }
    float s0 = 0.f, s1 = 0.f;
#pragma unroll
    for (int i = 0; i < 16; ++i) { s0 += __uint_as_float(v[i][0]); s1 += __uint_as_float(v[i][1]); }
    const float o0 = __shfl_xor(s0, 1), o1 = __shfl_xor(s1, 1);
    s0 = ph ? o0 + s0 : s0 + o0; s1 = ph ? o1 + s1 : s1 + o1;
    const unsigned pk = pk_bf16(s0, s1);
    if (gi < 105 && ph == 0) {
      if (e == 20) {
        __hip_atomic_store((u64*)(ws + WS_Z) + hd * 10 + 5 * half + dp, ((u64)tag << 32) | (u64)pk, __ATOMIC_RELAXED, __HIP_MEMORY_SCOPE_AGENT);
      } else {
        unsigned short* kt = (unsigned short*)(smem + 40960);
        kt[(2 * dp) * 32 + e] = (unsigned short)(pk & 0xffff);
        kt[(2 * dp + 1) * 32 + e] = (unsigned short)(pk >> 16);
      }
    }
  }
  bsync();
  STAMP(2);
  {
    const int q = r16 >> 2, p = lane & 3;
    unsigned trb[2];
#pragma unroll
    for (int t = 0; t < 2; ++t) { int k = 8 * h + 4 * t + q; trb[t] = (k ^ (((k >> 3) & 1) << 2)) * 160 + p * 8; }
    const s16x8 kb = *(const s16x8*)(smem + 40960 + r16 * 64 + h * 16);
    char* mst = smem + 45056;
#pragma unroll
    for (int nt = 0; nt < 10; ++nt) {
      const int g8 = 2 * w + nt / 5, t5 = nt % 5;
      const unsigned o = g8 * 5120 + t5 * 32;
      const s16x8 wf = cat8(tr_read(smem, o + trb[0]), tr_read(smem, o + trb[1]));
      f32x4 dacc = {0.f, 0.f, 0.f, 0.f};
      dacc = mfma16(wf, kb, dacc);
      if (r16 < 10) *(u32x2*)(mst + r16 * 1296 + (80 * g8 + 16 * t5 + 4 * h) * 2) = u32x2{pk_bf16(dacc[0], dacc[1]), pk_bf16(dacc[2], dacc[3])};
    }
    bsync();
    STAMP(3);
#pragma unroll
    for (int i = 0; i < 4; ++i) {
      const int c = tid + 256 * i;
      if (c < 800) {
        const int d = c / 80, cc = c - d * 80, g8 = cc / 10, ch = cc - g8 * 10;
        const int f = 20 * hd + 10 * half + d, k = f & 63;
        st16_sc1(ws, WS_MT + (size_t)(g8 * 5 + (f >> 7)) * SLOT + ((f >> 6) & 1) * 10240 + (k ^ (((k >> 3) & 1) << 2)) * 160 + ch * 16,
                 *(const u32x4*)(mst + d * 1296 + cc * 16));
      }
    }
    wait_v<0>();
    wait_l_bar();
    if (tid == 0) flag_set(ws, WS_F2 + (size_t)rb * 128, tag);
    STAMP(9);
  }
  if (rb == 0 && w == 0) {
    flags_wait(ws, WS_F2, 64, tag);
#pragma unroll 1
    for (int i = 0; i < 5; ++i) flags_wait(ws, WS_F4 + (size_t)i * 64 * 128, 64, tag);
    if (lane == 0) st4_sc1(ws + WS_EPOCH, tag);
  }
}

struct cplx { float r, i; };
DI cplx cmul(cplx a, cplx b) { return cplx{a.r * b.r - a.i * b.i, a.r * b.i + a.i * b.r}; }
DI cplx cexp_at(float ar, float ai, float p) {
  float m = __expf(ar * p);
  float x = ai * p;
  float k = rintf(x * 0.15915494309189535f);
  float r = fmaf(-k, 6.2831854820251465f, x);
  r = fmaf(-k, -1.7484556000744487e-07f, r);
  float rv = r * 0.15915494309189535f;
  return cplx{m * __builtin_amdgcn_cosf(rv), m * __builtin_amdgcn_sinf(rv)};
}
constexpr int KG_PITCH = 288;
constexpr int KG_WAVE = 57 * KG_PITCH;
constexpr int CV_KP = 2240;
constexpr int CV_U = 4 * CV_KP;
constexpr int CV_WAVE = CV_U + 65 * 80 + 48;
constexpr int B_T = 4 * KG_WAVE;
constexpr int B_RAW = 4 * CV_WAVE;
constexpr int B_T2 = B_RAW + 16384;

DI void role_B(char* smem, int bb, const unsigned short* __restrict__ u, const unsigned short* __restrict__ lar_,
               const unsigned short* __restrict__ aim_, const unsigned short* __restrict__ cr_,
               const unsigned short* __restrict__ ci_, const unsigned short* __restrict__ ldt_,
               const unsigned short* __restrict__ dsk_, char* __restrict__ ws, unsigned short* __restrict__ out) {
  const int tid = threadIdx.x, w = tid >> 6, lane = tid & 63, r16 = lane & 15, h4 = lane >> 4;
  const int d0 = ((bb & 7) * 20 + (bb >> 3)) * 4, d = d0 + w;
  STAMP_DECL
  STAMP(0);
  const unsigned tag = __hip_atomic_load((const unsigned*)(ws + WS_EPOCH), __ATOMIC_RELAXED, __HIP_MEMORY_SCOPE_AGENT) + 1u;
  u32x4 tv[4];
#pragma unroll
  for (int i = 0; i < 4; ++i) {
    const int t = 2 * bb + (i >> 1), idx = tid + 256 * (i & 1), r = idx >> 3, c8 = idx & 7;
    tv[i] = *(const u32x4*)(u + (size_t)((t / 10) * 64 + r) * DM + (t % 10) * 64 + c8 * 8);
  }
  STAMP(1);
  float inv_scale;
  {
    char* sm = smem + w * KG_WAVE;
    float lar[3], aim[3], crv[3], civ[3];
#pragma unroll
    for (int c = 0; c < 3; ++c) {
      const int n = d * NMODE + 64 * c + lane;
      lar[c] = bf2f(lar_[n]); aim[c] = bf2f(aim_[n]); crv[c] = bf2f(cr_[n]); civ[c] = bf2f(ci_[n]);
    }
    const float dt = bfr(expf(bf2f(ldt_[d])));
    float s = 0.f;
#pragma unroll
    for (int c = 0; c < 3; ++c) { crv[c] *= dt; civ[c] *= dt; s += fabsf(crv[c]) + fabsf(civ[c]); }
#pragma unroll
    for (int o = 32; o >= 1; o >>= 1) s += __shfl_xor(s, o);
    const float S = 2.f * s;
    int es = (int)((__float_as_uint(S) >> 23) & 0xff);
    int se = 268 - es; se = se < 1 ? 1 : (se > 253 ? 253 : se);
    const float sc = __uint_as_float((unsigned)se << 23);
    inv_scale = __uint_as_float((unsigned)(254 - se) << 23);
    f32x4 acc[3];
#pragma unroll
    for (int t = 0; t < 3; ++t) acc[t] = f32x4{0.f, 0.f, 0.f, 0.f};
    char* wp = sm + 4 * lane;
    const char* ap = sm + r16 * KG_PITCH + h4 * 16;
#pragma unroll 1
    for (int c = 0; c < DBG_NCH; ++c) {
      const float larc = c == 0 ? lar[0] : (c == 1 ? lar[1] : lar[2]);
      const float aimc = c == 0 ? aim[0] : (c == 1 ? aim[1] : aim[2]);
      const float crc = c == 0 ? crv[0] : (c == 1 ? crv[1] : crv[2]);
      const float cic = c == 0 ? civ[0] : (c == 1 ? civ[1] : civ[2]);
      const float a = bfr(expf(larc));
      const float ar = -a * dt, ai = aimc * dt;
      const cplx cc = cplx{crc * sc, cic * sc};
      auto putA = [&](int row, cplx v) AINL { *(unsigned*)(wp + row * KG_PITCH) = pk_f16(v.r, v.i); };
      auto putB = [&](int row, cplx v) AINL { *(unsigned*)(wp + (41 + row) * KG_PITCH) = pk_f16(v.r, v.i); };
      {
        cplx f[16];
        f[0] = cplx{1.f, 0.f}; f[1] = cexp_at(ar, -ai, 8.f);
        f[2] = cmul(f[1], f[1]); f[3] = cmul(f[2], f[1]); f[4] = cmul(f[2], f[2]);
        f[5] = cmul(f[4], f[1]); f[6] = cmul(f[4], f[2]); f[7] = cmul(f[4], f[3]); f[8] = cmul(f[4], f[4]);
#pragma unroll
        for (int j = 1; j < 8; ++j) f[8 + j] = cmul(f[8], f[j]);
#pragma unroll
        for (int j = 0; j < 16; ++j) putB(j, f[j]);
      }
      cplx e[8], g[8], cg[17];
      e[0] = cplx{1.f, 0.f}; e[1] = cexp_at(ar, ai, 1.f);
      e[2] = cmul(e[1], e[1]); e[3] = cmul(e[2], e[1]); e[4] = cmul(e[2], e[2]);
      e[5] = cmul(e[4], e[1]); e[6] = cmul(e[4], e[2]); e[7] = cmul(e[4], e[3]);
      g[0] = cplx{1.f, 0.f}; g[1] = cexp_at(ar, ai, 128.f);
      g[2] = cmul(g[1], g[1]); g[3] = cmul(g[2], g[1]); g[4] = cmul(g[2], g[2]);
      g[5] = cmul(g[4], g[1]); g[6] = cmul(g[4], g[2]); g[7] = cmul(g[4], g[3]);
      const cplx g8 = cexp_at(ar, ai, 1024.f);
      cg[0] = cc; cg[8] = cmul(cc, g8); cg[16] = cmul(cg[8], g8);
#pragma unroll
      for (int j = 1; j < 8; ++j) { cg[j] = cmul(cc, g[j]); cg[8 + j] = cmul(cg[8], g[j]); }
#pragma unroll
      for (int i = 0; i < 16; ++i) putA(i, (i & 7) ? cmul(cg[i >> 3], e[i & 7]) : cg[i >> 3]);
#pragma unroll
      for (int i = 0; i < 8; ++i) putA(16 + i, (i & 3) ? cmul(cg[2 + (i >> 2)], e[2 * (i & 3)]) : cg[2 + (i >> 2)]);
#pragma unroll
      for (int i = 0; i < 8; ++i) putA(24 + i, (i & 1) ? cmul(cg[4 + (i >> 1)], e[4]) : cg[4 + (i >> 1)]);
#pragma unroll
      for (int i = 0; i < 8; ++i) putA(32 + i, cg[8 + i]);
      putA(40, cg[16]);
      s16x8 bf[4], af[4][3];
#pragma unroll
      for (int ks = 0; ks < 4; ++ks) {
        bf[ks] = *(const s16x8*)(ap + 41 * KG_PITCH + ks * 64);
#pragma unroll
        for (int t = 0; t < 3; ++t) af[ks][t] = *(const s16x8*)(ap + 16 * t * KG_PITCH + ks * 64);
      }
#pragma unroll
      for (int ks = 0; ks < 4; ++ks)
#pragma unroll
        for (int t = 0; t < 3; ++t) acc[t] = mfma16h(af[ks][t], bf[ks], acc[t]);
      if (c == 0) {
        unsigned short* t_ = (unsigned short*)smem;
        bsync();
#pragma unroll
        for (int i = 0; i < 4; ++i) {
          const int idx = tid + 256 * (i & 1), r = idx >> 3, c8 = idx & 7;
#pragma unroll
          for (int j = 0; j < 4; ++j) *(unsigned*)(t_ + (i >> 1) * 4224 + r * 66 + c8 * 8 + 2 * j) = tv[i][j];
        }
        bsync();
#pragma unroll
        for (int i = 0; i < 4; ++i) {
          const int t = 2 * bb + (i >> 1), idx = tid + 256 * (i & 1), cc = idx >> 3, r8 = idx & 7;
          const unsigned short* tt = t_ + (i >> 1) * 4224;
          u32x4 o;
#pragma unroll
          for (int j = 0; j < 4; ++j) o[j] = (unsigned)tt[(r8 * 8 + 2 * j) * 66 + cc] | ((unsigned)tt[(r8 * 8 + 2 * j + 1) * 66 + cc] << 16);
          st16_sc1(ws, WS_UT + ((size_t)((t % 10) * 64 + cc) * SEQ + (t / 10) * 64 + r8 * 8) * 2, o);
        }
        wait_l_bar();
      }
      if (c == 1) {
        wait_v<0>();
        __builtin_amdgcn_s_barrier();
        if (tid < 2) { const int t = 2 * bb + tid; flag_set(ws, WS_F4 + (size_t)((t % 10) * 32 + t / 10) * 128, tag); }
      }
    }
    unsigned short* T = (unsigned short*)(smem + B_T2 + w * 1408);
#pragma unroll
    for (int t = 0; t < 3; ++t)
#pragma unroll
      for (int rg = 0; rg < 4; ++rg) {
        const int i = 4 * h4 + rg, j = r16;
        int idx;
        if (t == 0) idx = (i & 7) + 128 * (i >> 3) + 8 * j;
        else if (t == 1) idx = i < 8 ? 256 + (i & 3) + 64 * ((i >> 2) & 1) + 4 * j : 384 + (i & 1) + 32 * ((i >> 1) & 3) + 2 * j;
        else idx = i < 8 ? 512 + 16 * i + j : ((i == 8 && j == 0) ? 640 : 700);
        T[idx] = (unsigned short)(pk_f16(2.f * acc[t][rg], 0.f) & 0xffff);
      }
  }
  STAMP(2);
  bsync();
  flags_wait(ws, WS_F4 + (size_t)((d0 >> 6) * 32) * 128, 32, tag);
  STAMP(3);
  char* cb = smem + w * CV_WAVE;
  const unsigned coff = w * CV_WAVE;
  float uinv;
  {
    u32x4 uv[4];
    unsigned mx = 0;
#pragma unroll
    for (int c = 0; c < 4; ++c) uv[c] = *(const u32x4*)(ws + WS_UT + (size_t)d * SEQ * 2 + (lane + 64 * c) * 16);
    {
      const char* T = smem + B_T2 + w * 1408;
      char* k16 = smem + B_RAW + w * 4096;
#pragma unroll
      for (int c = 0; c < 4; ++c) {
        const int a = lane + 64 * c;
        u32x4 o;
        if (c == 0) {
          if (lane < 32) o = *(const u32x4*)(T + a * 16);
          else {
            const u32x2 e = *(const u32x2*)(T + 256 + a * 8);
            const unsigned e4 = *(const unsigned short*)(T + 256 + a * 8 + 8);
            o = u32x4{(e[0] & 0xffffu) * 0x10001u, (e[0] >> 16) | (e[1] << 16), (e[1] & 0xffffu) * 0x10001u, (e[1] >> 16) | (e4 << 16)};
          }
        } else if (c == 1) {
          const unsigned e = *(const unsigned*)(T + 512 + a * 4);
          const unsigned e2 = *(const unsigned short*)(T + 512 + a * 4 + 4);
          o = u32x4{(e & 0xffffu) * 0x10001u, e, (e >> 16) * 0x10001u, e2 * 0x10001u};
        } else {
          const unsigned e0 = *(const unsigned short*)(T + 768 + a * 2), e1 = *(const unsigned short*)(T + 770 + a * 2);
          o = u32x4{e0 * 0x10001u, e0 * 0x10001u, ((a & 1) ? e1 : e0) | (e1 << 16), e1 * 0x10001u};
        }
        *(u32x4*)(k16 + a * 16) = o;
      }
    }
#pragma unroll
    for (int c = 0; c < 4; ++c) {
#pragma unroll
      for (int j = 0; j < 4; ++j) mx = max(mx, max(uv[c][j] & 0x7fffu, (uv[c][j] >> 16) & 0x7fffu));
    }
#pragma unroll
    for (int o = 32; o >= 1; o >>= 1) mx = max(mx, (unsigned)__shfl_xor((int)mx, o));
    int eu = (int)(mx >> 7);
    int se = 268 - eu; se = se > 253 ? 253 : (se < 1 ? 1 : se);
    const float usc = __uint_as_float((unsigned)se << 23);
    uinv = __uint_as_float((unsigned)(254 - se) << 23);
#pragma unroll
    for (int c = 0; c < 4; ++c) {
      const int cidx = lane + 64 * c;
      u32x4 o4;
#pragma unroll
      for (int j = 0; j < 4; ++j) o4[j] = pk_f16(lo16(uv[c][j]) * usc, hi16(uv[c][j]) * usc);
      *(u32x4*)(cb + CV_U + ((cidx >> 2) + 1) * 80 + (cidx & 3) * 16) = o4;
    }
    if (lane < 5) *(u32x4*)(cb + CV_U + lane * 16) = u32x4{0u, 0u, 0u, 0u};
  }
  STAMP(4);
  const char* k16 = smem + B_RAW + w * 4096;
  const int i32 = lane & 31, h = lane >> 5, blk = (lane >> 4) & 1, q = (lane & 15) >> 2, p = lane & 3;
  const unsigned ab = coff + q * CV_KP + 2 * (32 + 16 * blk - 8 * h + 4 * p) - 40;
  const unsigned ubase = coff + CV_U + 16 * h;
  f32x16 acc0 = {}, acc1 = {};
#pragma unroll
  for (int pass = 0; pass < DBG_NPASS; ++pass) {
#pragma unroll
    for (int it = 0; it < 3; ++it) {
      const int qc = lane + 64 * it;
      if (qc < 136) {
        const int seg = qc >= 68;
        const int t0 = 1024 * seg + 512 * pass - 32 + 8 * (qc - 68 * seg);
        u32x4 a = {0u, 0u, 0u, 0u};
        u32x2 bb2 = {0u, 0u};
        if (t0 >= 0) a = *(const u32x4*)(k16 + t0 * 2);
        if (t0 >= 4) bb2 = *(const u32x2*)(k16 + t0 * 2 - 8);
        const unsigned s03 = __builtin_amdgcn_alignbit(a[0], bb2[1], 16), s01 = __builtin_amdgcn_alignbit(a[1], a[0], 16),
                       s12 = __builtin_amdgcn_alignbit(a[2], a[1], 16), s23 = __builtin_amdgcn_alignbit(a[3], a[2], 16),
                       sb = __builtin_amdgcn_alignbit(bb2[1], bb2[0], 16);
        char* dst = cb + 16 * qc;
        *(u32x4*)(dst) = a;
        *(u32x4*)(dst + CV_KP) = u32x4{s03, s01, s12, s23};
        *(u32x4*)(dst + 2 * CV_KP) = u32x4{opq(bb2[1]), opq(a[0]), opq(a[1]), opq(a[2])};
        *(u32x4*)(dst + 3 * CV_KP) = u32x4{sb, s03, s01, s12};
      }
    }
    s16x8 fa[4][8];
    auto ldfr = [&](int ml, s16x8 (&f)[8]) AINL {
      const int m = 16 * pass + ml;
      const unsigned a0 = ab + 64 * ml;
      const int r0 = i32 - m + 1;
      const unsigned u0 = ubase + 80 * (r0 > 0 ? r0 : 0);
      const unsigned u1 = ubase + 80 * (i32 - m + 33);
#pragma unroll
      for (int ks = 0; ks < 2; ++ks) {
        const unsigned ao = a0 + 32 * (1 - ks);
        f[ks] = cat8(tr_read(smem, ao + 8), tr_read(smem, ao));
        f[2 + ks] = cat8(tr_read(smem, ao + 1088 + 8), tr_read(smem, ao + 1088));
        f[4 + ks] = *(const s16x8*)(smem + u0 + 32 * ks);
        f[6 + ks] = *(const s16x8*)(smem + u1 + 32 * ks);
      }
    };
    auto mm = [&](s16x8 (&f)[8]) AINL {
#pragma unroll
      for (int ks = 0; ks < 2; ++ks) {
        acc0 = mfma32h(f[ks], f[4 + ks], acc0);
        acc1 = mfma32h(f[ks], f[6 + ks], acc1);
        acc1 = mfma32h(f[2 + ks], f[4 + ks], acc1);
      }
    };
    STAMP(5 + 2 * pass);
    ldfr(0, fa[0]);
    ldfr(1, fa[1]);
#define CV_REGION(MLD, BLD, BMM) do { \
      ldfr((MLD), fa[BLD]); mm(fa[BMM]); \
      _Pragma("unroll") for (int i_ = 0; i_ < 6; ++i_) { \
        __builtin_amdgcn_sched_group_barrier(0x008, 1, 0); __builtin_amdgcn_sched_group_barrier(0x100, 2, 0); } \
      __builtin_amdgcn_sched_barrier(0); } while (0)
#pragma unroll 1
    for (int m = 0; m < 16; m += 4) {
      CV_REGION(m + 2, 2, 0);
      CV_REGION(m + 3, 3, 1);
      CV_REGION((m + 4) & 15, 0, 2);
      CV_REGION((m + 5) & 15, 1, 3);
    }
#undef CV_REGION
    STAMP(6 + 2 * pass);
  }
  {
    const float inv = inv_scale * uinv;
    const float dk = bf2f(dsk_[d]);
    char* raw = smem + B_RAW + w * 4096;
#pragma unroll
    for (int c = 0; c < 2; ++c)
#pragma unroll
      for (int g4 = 0; g4 < 4; ++g4) {
        const int l = 1024 * c + 32 * i32 + 8 * g4 + 4 * h;
        const u32x2 uu = *(const u32x2*)(cb + CV_U + ((l >> 5) + 1) * 80 + (l & 31) * 2);
        float r[4];
#pragma unroll
        for (int j = 0; j < 4; ++j) {
          const float y = bfr((c ? acc1[4 * g4 + j] : acc0[4 * g4 + j]) * inv);
          const unsigned short hb = (unsigned short)((j & 1) ? (uu[j >> 1] >> 16) : (uu[j >> 1] & 0xffffu));
          const float uf = (float)__builtin_bit_cast(_Float16, hb) * uinv;
          r[j] = y + bfr(uf * dk);
        }
        *(u32x2*)(raw + l * 2) = u32x2{pk_bf16(r[0], r[1]), pk_bf16(r[2], r[3])};
      }
  }
  bsync();
  {
    const unsigned short* rt_ = (const unsigned short*)(smem + B_RAW);
#pragma unroll
    for (int i = 0; i < 8; ++i) {
      const int l = tid + 256 * i;
      const u32x2 v = {(unsigned)rt_[l] | ((unsigned)rt_[2048 + l] << 16), (unsigned)rt_[4096 + l] | ((unsigned)rt_[6144 + l] << 16)};
      *(u32x2*)(out + (size_t)l * (2 * DM) + DM + d0) = v;
    }
  }
  STAMP(9);
}

__global__ NOPK __launch_bounds__(256, 2) void k_fused(
    const unsigned short* __restrict__ x, const unsigned short* __restrict__ wqkv, const unsigned short* __restrict__ wo,
    const unsigned short* __restrict__ u, const unsigned short* __restrict__ lar, const unsigned short* __restrict__ aim,
    const unsigned short* __restrict__ cr, const unsigned short* __restrict__ ci, const unsigned short* __restrict__ ldt,
    const unsigned short* __restrict__ dsk, char* __restrict__ ws, unsigned short* __restrict__ out) {
  extern __shared__ __attribute__((aligned(16))) char smem[];
  const int b = blockIdx.x;
  if (b < NB_A) role_A(smem, b, x, wqkv, wo, ws, out);
  else if (b < NB_A + NB_R) role_R(smem, b - NB_A, wo, ws);
  else {
    role_B(smem, b - NB_A - NB_R, u, lar, aim, cr, ci, ldt, dsk, ws, out);
  }
}

extern "C" void kernel_launch(void* const* d_in, const int* in_sizes, int n_in, void* d_out, int out_size, void* d_ws,
                              size_t ws_size, hipStream_t stream) {
  constexpr int LDS = 81920;
  (void)hipFuncSetAttribute((const void*)k_fused, hipFuncAttributeMaxDynamicSharedMemorySize, LDS);
  k_fused<<<NB_A + NB_R + NB_B, 256, LDS, stream>>>(
      (const unsigned short*)d_in[0], (const unsigned short*)d_in[1], (const unsigned short*)d_in[2],
      (const unsigned short*)d_in[3], (const unsigned short*)d_in[4], (const unsigned short*)d_in[5],
      (const unsigned short*)d_in[6], (const unsigned short*)d_in[7], (const unsigned short*)d_in[8],
      (const unsigned short*)d_in[9], (char*)d_ws, (unsigned short*)d_out);
}
```
